```python
import torch

def _softmax(x, axis=-1):
    return torch.softmax(x, dim=axis)
def _gelu(x):
    return torch.nn.functional.gelu(x, approximate="tanh")
def _silu(x):
    return torch.nn.functional.silu(x)
def _sigmoid(x):
    return torch.sigmoid(x)
def _layernorm(x, axis=-1, eps=1e-5):
    m = x.mean(dim=axis, keepdim=True)
    v = x.var(dim=axis, keepdim=True, unbiased=False)
    return (x - m) / torch.sqrt(v + eps)
def _scatter_add(tgt, idx, src, axis=0):
    return tgt.clone().index_add_(axis, idx, src)
def _segment_sum(src, seg, n):
    out = torch.zeros((n,) + src.shape[1:], dtype=src.dtype)
    return out.index_add_(0, seg, src)
def _pad(x, pw):
    flat = tuple(v for pair in reversed(pw) for v in pair)
    return torch.nn.functional.pad(x, flat)

kernel_name = 'gated_mix__gqa_block_cached__geglu_ffn__decode'

def setup_inputs():

    _w = (lambda a: (lambda t: t.to(torch.bfloat16) if t.is_floating_point() else t)(torch.as_tensor(a)))
    def _A_setup():
        B, Sq, D = 8, 1, 512
        Hq, Hkv, Sk, Dh = 32, 8, 8192, 128
        assert Hq % Hkv == 0, (Hq, Hkv)
        return {
            "x": _w((lambda *s: torch.randn(*s, dtype=torch.bfloat16) * 0.05)(B, Sq, D)),
            "k_cache": _w((lambda *s: torch.randn(*s, dtype=torch.bfloat16) * 6.4)(B, Hkv, Sk, Dh)),
            "v_cache": _w((lambda *s: torch.randn(*s, dtype=torch.bfloat16) * 0.2)(B, Hkv, Sk, Dh)),
            "wq": _w((lambda *s: torch.randn(*s, dtype=torch.bfloat16) * 6.4)(D, Hq * Dh) * (D ** -0.5)),
            "wo": _w((lambda *s: torch.randn(*s, dtype=torch.bfloat16) * 0.2)(Hq * Dh, D) * ((Hq * Dh) ** -0.5)),
        }
    _A_in = _A_setup()

    def _B_setup():
        B, S, D, F = 8, 1, 512, 1792
        return {
            "x": _w((lambda *s: torch.randn(*s, dtype=torch.bfloat16) * 0.05)(B, S, D)),
            "w_gate": _w((lambda *s: torch.randn(*s, dtype=torch.bfloat16) * 0.05)(D, F)),
            "w_up": _w((lambda *s: torch.randn(*s, dtype=torch.bfloat16) * 0.05)(D, F)),
            "w_down": _w((lambda *s: torch.randn(*s, dtype=torch.bfloat16) * 0.05)(F, D)),
        }
    _B_in = _B_setup()

    return {
        "A_x": _A_in["x"],
        "A_k_cache": _A_in["k_cache"],
        "A_v_cache": _A_in["v_cache"],
        "A_wq": _A_in["wq"],
        "A_wo": _A_in["wo"],
        "B_x": _B_in["x"],
        "B_w_gate": _B_in["w_gate"],
        "B_w_up": _B_in["w_up"],
        "B_w_down": _B_in["w_down"],
        "gate_w": _w((lambda *s: torch.randn(*s, dtype=torch.bfloat16) * 0.05)(512) * 16.0),
    }

def reference(A_x, A_k_cache, A_v_cache, A_wq, A_wo, B_x, B_w_gate, B_w_up, B_w_down, gate_w):
    def _A_ref(x, k_cache, v_cache, wq, wo):
        B, Sq, D = x.shape
        Hq, Dh = 32, 128
        Hkv = k_cache.shape[1]
        rep = Hq // Hkv
        q = (lambda x, *d: x.permute(*d))((x @ wq).reshape(B, Sq, Hq, Dh), 0, 2, 1, 3)
        k = (lambda x, n, axis: torch.repeat_interleave(x, n, dim=axis))(k_cache, rep, axis=1)
        v = (lambda x, n, axis: torch.repeat_interleave(x, n, dim=axis))(v_cache, rep, axis=1)
        scores = torch.einsum("bhqd,bhkd->bhqk", q, k) * (Dh ** -0.5)
        p = _softmax(scores, axis=-1)
        o = torch.einsum("bhqk,bhkd->bhqd", p, v)
        return (lambda x, *d: x.permute(*d))(o, 0, 2, 1, 3).reshape(B, Sq, Hq * Dh) @ wo

    def _B_ref(x, w_gate, w_up, w_down):
        return (_gelu(x @ w_gate) * (x @ w_up)) @ w_down

    x = A_x
    g = _sigmoid((x * gate_w).sum(-1, keepdims=True))
    a = _A_ref(x, A_k_cache, A_v_cache, A_wq, A_wo)
    b = _B_ref(x, B_w_gate, B_w_up, B_w_down)
    return g * a + (1 - g) * b
```

```cpp
#include <hip/hip_runtime.h>
#include <hip/hip_bf16.h>
#include <stdint.h>

#define GAS __attribute__((address_space(1)))
#define LAS __attribute__((address_space(3)))
typedef unsigned v4u __attribute__((ext_vector_type(4)));
typedef unsigned v2u __attribute__((ext_vector_type(2)));
typedef float f32x4 __attribute__((ext_vector_type(4)));
typedef float f32x2 __attribute__((ext_vector_type(2)));
typedef __bf16 bf16x8 __attribute__((ext_vector_type(8)));
typedef short s16x4 __attribute__((ext_vector_type(4)));
typedef unsigned long long u64;
typedef GAS u64 gu64;
#define RLX_AGENT __ATOMIC_RELAXED, __HIP_MEMORY_SCOPE_AGENT

namespace {
constexpr int NB = 8, DM = 512, HQ = 32, HKV = 8, SK = 8192, DH = 128, FF = 1792;
constexpr int NWG = 256, NCONS = 4, NTHR = (1 + NCONS) * 64;
constexpr int NLAP = 8;
constexpr int SLOT = 16384;
constexpr int L_RING = 0, L_A = 8 * SLOT, L_B = L_A + SLOT, L_Q = L_B + 8192, L_CTRL = L_Q + 1024, L_BYTES = L_CTRL + 512;
constexpr int C_LANDED = 0, C_DONE = 4, C_GUFREE = 8, C_QREADY = 9, C_CLEAN = 10, C_TAGOK = 11, C_MRG = 12, C_RED = 16, C_TAG = 20, C_G = 24;
static_assert(L_BYTES <= 160 * 1024, "LDS");
constexpr size_t W_EPOCH = 0, W_FLAG = 4096, W_QG = 8192, W_H = W_QG + (size_t)NB * 4096 * 8;
constexpr int HP = 1856;
constexpr size_t W_PG = W_H + 32768;
constexpr int PGH = 72;
constexpr size_t W_OG = W_PG + (size_t)NWG * 4 * PGH * 8;
constexpr size_t W_END = W_OG + (size_t)HQ * NB * DM * 8;
static_assert((size_t)NB * HP * 2 <= 32768, "h");
constexpr u64 EPOCH_STEP = 0x9E3779B97F4A7C15ull;
constexpr unsigned GSPIN = 1u << 15, LSPIN = 1u << 18;
constexpr float SC2 = 0.08838834764831845f * 1.4426950408889634f;

struct Args {
  const unsigned char *x, *k, *v, *wq, *wo, *wg, *wu, *wd, *gw;
  unsigned char *out, *ws;
};

__device__ __forceinline__ f32x4 mfma(v4u a, v4u b, f32x4 c) {
  return __builtin_amdgcn_mfma_f32_16x16x32_bf16(__builtin_bit_cast(bf16x8, a), __builtin_bit_cast(bf16x8, b), c, 0, 0, 0);
}
__device__ __forceinline__ v2u trread(LAS unsigned char* p) {
  return __builtin_bit_cast(v2u, __builtin_amdgcn_ds_read_tr16_b64_v4i16((LAS s16x4*)p));
}
__device__ __forceinline__ unsigned cvtpk(float lo, float hi) {
  typedef __bf16 bf16x2 __attribute__((ext_vector_type(2)));
  bf16x2 r; r[0] = (__bf16)lo; r[1] = (__bf16)hi; return __builtin_bit_cast(unsigned, r);
}
template <int CTRL> __device__ __forceinline__ float dpp(float x) {
  return __builtin_bit_cast(float, __builtin_amdgcn_mov_dpp(__builtin_bit_cast(int, x), CTRL, 0xf, 0xf, true));
}
__device__ __forceinline__ float xrow_max(float x) {
  auto s = __builtin_amdgcn_permlane16_swap(__float_as_uint(x), __float_as_uint(x), false, false);
  x = fmaxf(__uint_as_float(s[0]), __uint_as_float(s[1]));
  auto t = __builtin_amdgcn_permlane32_swap(__float_as_uint(x), __float_as_uint(x), false, false);
  return fmaxf(__uint_as_float(t[0]), __uint_as_float(t[1]));
}
__device__ __forceinline__ float xrow_sum(float x) {
  auto s = __builtin_amdgcn_permlane16_swap(__float_as_uint(x), __float_as_uint(x), false, false);
  x = __uint_as_float(s[0]) + __uint_as_float(s[1]);
  auto t = __builtin_amdgcn_permlane32_swap(__float_as_uint(x), __float_as_uint(x), false, false);
  return __uint_as_float(t[0]) + __uint_as_float(t[1]);
}
__device__ __forceinline__ float bflo(unsigned u) { return __uint_as_float(u << 16); }
__device__ __forceinline__ float bfhi(unsigned u) { return __uint_as_float(u & 0xffff0000u); }

#define CBAR() asm volatile("" ::: "memory")
#define LDS_WAIT() asm volatile("s_waitcnt lgkmcnt(0)" ::: "memory")
#define DMA4(sbase, dst, v0, v1, v2, v3)                                                                         \
  asm volatile("s_mov_b32 m0, %5\n\ts_nop 0\n\t"                                                                 \
               "global_load_lds_dwordx4 %0, %4 nt\n\t"                                                           \
               "global_load_lds_dwordx4 %1, %4 offset:1024 nt\n\t"                                               \
               "global_load_lds_dwordx4 %2, %4 offset:2048 nt\n\t"                                               \
               "global_load_lds_dwordx4 %3, %4 offset:3072 nt"                                                   \
               :: "v"(v0), "v"(v1), "v"(v2), "v"(v3), "s"(sbase), "s"(dst) : "memory")
#define DMA1S(sbase, voff, dst)                                                                                  \
  asm volatile("s_mov_b32 m0, %2\n\ts_nop 0\n\tglobal_load_lds_dwordx4 %0, %1 nt" :: "v"(voff), "s"(sbase), "s"(dst) : "memory")
#define DMA1V(vaddr, dst)                                                                                        \
  asm volatile("s_mov_b32 m0, %1\n\ts_nop 0\n\tglobal_load_lds_dwordx4 %0, off nt" :: "v"(vaddr), "s"(dst) : "memory")
#define VMWAIT(n) asm volatile("s_waitcnt vmcnt(" #n ")" ::: "memory")

__device__ __forceinline__ void lds_wait_ge(volatile LAS unsigned* w, unsigned need) {
  unsigned sp = 0;
  while (*w < need && ++sp < LSPIN) __builtin_amdgcn_s_sleep(1);
  CBAR();
}

__global__ void __launch_bounds__(NTHR, 2) fused_kernel(Args a) {
  extern __shared__ __attribute__((aligned(16))) unsigned char smem[];
  LAS unsigned char* L = (LAS unsigned char*)smem;
  volatile LAS unsigned* CT = (volatile LAS unsigned*)(L + L_CTRL);
  const int tid = threadIdx.x, lane = tid & 63, wave = __builtin_amdgcn_readfirstlane(tid >> 6);
  const int bid = blockIdx.x;
  const int kap = bid >> 5, jg = bid & 31, b = jg >> 2, s = jg & 3;
  const int qcol = kap * 512 + (((jg & 7) << 2) | (jg >> 3)) * 16;
  const bool has_ffn = (bid >> 3) < 28;
  const int fsl = (bid & 7) * 28 + (has_ffn ? (bid >> 3) : 27);
  const int hh = jg >> 3, ht = kap * 4 + hh, n8 = jg & 7;
  const int g = lane >> 4, r = lane & 15;

  if (tid < 128) ((LAS unsigned*)(L + L_CTRL))[tid] = 0u;
  if (tid >= 64 && tid < 128) ((LAS v4u*)(L + L_B + 7168))[tid - 64] = v4u{0u, 0u, 0u, 0u};
  __syncthreads();

  if (wave == 0) {
    __builtin_amdgcn_s_setprio(3);
    const unsigned lb = (unsigned)(size_t)L;
    {
      const unsigned voff = (unsigned)(lane >> 1) * 8192u + (unsigned)(lane & 1) * 16u;
      const unsigned char* sb = a.wq + (size_t)qcol * 2;
#pragma unroll
      for (int j = 0; j < 16; ++j) DMA1S(sb + (size_t)j * 32 * 8192, voff, lb + L_A + j * 1024);
    }
    {
      const unsigned char* p = ((lane & 1) ? a.wu : a.wg) + (size_t)(lane >> 1) * (FF * 2) + (size_t)fsl * 16;
#pragma unroll
      for (int j = 0; j < 16; ++j) DMA1V(p + (size_t)j * 32 * (FF * 2), lb + L_RING + 7 * SLOT + j * 1024);
    }
    unsigned ko[4], vo[2];
#pragma unroll
    for (int j = 0; j < 4; ++j) ko[j] = (unsigned)g * 256u + (unsigned)((r ^ g ^ (4 * j)) * 16);
#pragma unroll
    for (int j = 0; j < 2; ++j) vo[j] = (unsigned)g * 256u + (unsigned)((r ^ (2 * (4 * j + g))) * 16);
    const size_t seq_off = ((size_t)(b * HKV + kap) * SK + (size_t)s * 2048) * 256;
    const unsigned char* kb = a.k + seq_off;
    const unsigned char* vb = a.v + seq_off;
    VMWAIT(16); CT[C_LANDED] = 1u;
    unsigned pub = 1;
    for (int lap = 0; lap < NLAP; ++lap) {
#pragma unroll
      for (int hv = 0; hv < 2; ++hv) {
#pragma unroll
        for (int c = 0; c < 4; ++c) {
          const int f = 2 + lap * 8 + hv * 4 + c;
          volatile LAS unsigned* fw = (hv == 1 && c == 3 && lap == 0) ? (CT + C_GUFREE) : (CT + C_DONE + c);
          const unsigned need = (hv == 1 && c == 3 && lap == 0) ? 1u : (unsigned)(2 * lap - 1 + hv);
          if (lap > 0 || (hv == 1 && c == 3)) {
            if (*fw < need) {
              VMWAIT(0); CT[C_LANDED] = (unsigned)f; pub = f;
              lds_wait_ge(fw, need);
            }
            LDS_WAIT();
          }
          const unsigned char* sb = (hv ? vb : kb) + (size_t)(lap * 4 + c) * SLOT;
          const unsigned dst = lb + L_RING + (hv * 4 + c) * SLOT;
          if (hv == 0) { DMA4(sb, dst, ko[0], ko[1], ko[2], ko[3]); DMA4(sb + 4096, dst + 4096, ko[0], ko[1], ko[2], ko[3]); }
          else         { DMA4(sb, dst, vo[0], vo[1], vo[0], vo[1]); DMA4(sb + 4096, dst + 4096, vo[0], vo[1], vo[0], vo[1]); }
          VMWAIT(16);
          if (hv == 0) { DMA4(sb + 8192, dst + 8192, ko[0], ko[1], ko[2], ko[3]); DMA4(sb + 12288, dst + 12288, ko[0], ko[1], ko[2], ko[3]); }
          else         { DMA4(sb + 8192, dst + 8192, vo[0], vo[1], vo[0], vo[1]); DMA4(sb + 12288, dst + 12288, vo[0], vo[1], vo[0], vo[1]); }
          VMWAIT(16);
          if (pub < (unsigned)f) { CT[C_LANDED] = (unsigned)f; pub = f; }
        }
      }
    }
    {
      const unsigned voff = (unsigned)(lane >> 3) * 1024u + (unsigned)(lane & 7) * 16u;
      const unsigned char* sb = a.wo + (size_t)ht * 128 * 1024 + (size_t)n8 * 128;
#pragma unroll
      for (int j = 0; j < 8; ++j) DMA1S(sb + (size_t)j * 8192, voff, lb + L_A + j * 1024);
      VMWAIT(8); CT[C_LANDED] = 66u;
#pragma unroll
      for (int j = 8; j < 16; ++j) DMA1S(sb + (size_t)j * 8192, voff, lb + L_A + j * 1024);
      const unsigned char* sd = a.wd + (size_t)ht * 56 * 1024 + (size_t)n8 * 128;
#pragma unroll
      for (int j = 0; j < 7; ++j) DMA1S(sd + (size_t)j * 8192, voff, lb + L_B + j * 1024);
      VMWAIT(0); CT[C_LANDED] = 68u;
    }
    return;
  }

  const int c = wave - 1;
  gu64* ws64 = (gu64*)a.ws;
  u64 T64;
  if (c == 2) {
    T64 = __hip_atomic_load(ws64 + W_EPOCH / 8, RLX_AGENT) + EPOCH_STEP;
    if (lane == 0) { CT[C_TAG] = (unsigned)T64; CT[C_TAG + 1] = (unsigned)(T64 >> 32); LDS_WAIT(); CT[C_TAGOK] = 1u; }
    const int gb = lane >> 3, part = lane & 7;
    const GAS v4u* xp = (const GAS v4u*)(a.x + (size_t)gb * 1024 + part * 128);
    const GAS v4u* gp = (const GAS v4u*)(a.gw + part * 128);
    float acc = 0.f;
#pragma unroll
    for (int i = 0; i < 8; ++i) {
      const v4u xv = xp[i], gv = gp[i];
#pragma unroll
      for (int e = 0; e < 4; ++e) { acc = fmaf(bflo(xv[e]), bflo(gv[e]), acc); acc = fmaf(bfhi(xv[e]), bfhi(gv[e]), acc); }
    }
    acc += dpp<0xB1>(acc); acc += dpp<0x4E>(acc); acc += dpp<0x141>(acc);
    const float gate = 1.f / (1.f + __expf(-acc));
    if (part == 0) ((volatile LAS float*)(CT + C_G))[gb] = gate;
  } else {
    v4u xf[16];
    if (c <= 1) {
      const GAS v4u* xp = (const GAS v4u*)(a.x + (size_t)(r & 7) * 1024 + g * 16);
#pragma unroll
      for (int ks = 0; ks < 16; ++ks) xf[ks] = xp[ks * 4];
    }
    lds_wait_ge(CT + C_TAGOK, 1u);
    T64 = ((u64)CT[C_TAG + 1] << 32) | CT[C_TAG];
    if (c == 1) {
      const u64 inv = (u64)(~(unsigned)T64) << 32;
      gu64* pg = ws64 + W_PG / 8 + (size_t)bid * 4 * PGH;
#pragma unroll
      for (int i = 0; i < 5; ++i) if (i * 64 + lane < 4 * PGH) __hip_atomic_store(pg + i * 64 + lane, inv, RLX_AGENT);
      gu64* og = ws64 + W_OG / 8 + (size_t)ht * NB * DM + n8 * 64 + lane;
#pragma unroll
      for (int i = 0; i < NB; ++i) __hip_atomic_store(og + i * DM, inv, RLX_AGENT);
    }
    if (c <= 1) {
      lds_wait_ge(CT + C_LANDED, (unsigned)(c + 1));
      LAS unsigned char* sl = L + (c == 0 ? L_A : L_RING + 7 * SLOT) + (8 * g + (r >> 2)) * 32 + (r & 3) * 8;
      f32x4 d0 = {0.f, 0.f, 0.f, 0.f}, d1 = {0.f, 0.f, 0.f, 0.f};
#pragma unroll
      for (int ks = 0; ks < 16; ks += 2) {
        const v2u b0 = trread(sl + ks * 1024), b1 = trread(sl + ks * 1024 + 128);
        const v2u b2 = trread(sl + ks * 1024 + 1024), b3 = trread(sl + ks * 1024 + 1024 + 128);
        d0 = mfma(xf[ks], v4u{b0.x, b0.y, b1.x, b1.y}, d0);
        d1 = mfma(xf[ks + 1], v4u{b2.x, b2.y, b3.x, b3.y}, d1);
      }
      d0 += d1;
      if (c == 0) {
        const float v0 = g < 2 ? d0[0] : d0[2], v1 = g < 2 ? d0[1] : d0[3];
        const int b0 = 4 * (g & 1) + (g < 2 ? 0 : 2);
        const unsigned pk = cvtpk(v0, v1);
        gu64* qg = ws64 + W_QG / 8 + (size_t)b0 * 4096 + qcol + r;
        __hip_atomic_store(qg, (T64 << 16) | (pk & 0xffffu), RLX_AGENT);
        __hip_atomic_store(qg + 4096, (T64 << 16) | (pk >> 16), RLX_AGENT);
      } else {
        LDS_WAIT(); CT[C_GUFREE] = 1u;
        float hv[4];
#pragma unroll
        for (int e = 0; e < 4; ++e) {
          const float gt = d0[e], up = dpp<0x128>(d0[e]);
          const float u = 0.7978845608028654f * (gt + 0.044715f * gt * gt * gt);
          const float th = 1.f - 2.f / (__expf(2.f * u) + 1.f);
          hv[e] = 0.5f * gt * (1.f + th) * up;
        }
        {
          u64 hw[4];
#pragma unroll
          for (int e = 0; e < 4; ++e) {
            const float n1 = dpp<0x55>(hv[e]), n2 = dpp<0xAA>(hv[e]), n3 = dpp<0xFF>(hv[e]);
            hw[e] = ((u64)cvtpk(n2, n3) << 32) | cvtpk(hv[e], n1);
          }
          if (has_ffn && (r & 11) == 0 && g < 2) {
            gu64* hp = (gu64*)(a.ws + W_H + ((size_t)(4 * g) * HP + fsl * 8 + r) * 2);
#pragma unroll
            for (int e = 0; e < 4; ++e) __hip_atomic_store(hp + (size_t)e * (HP / 4), hw[e], RLX_AGENT);
          }
        }
        VMWAIT(0);
        if (lane == 0) __hip_atomic_store(ws64 + W_FLAG / 8 + bid, T64, RLX_AGENT);
      }
    } else {
      gu64* qg = ws64 + W_QG / 8 + (size_t)b * 4096 + kap * 512 + lane;
      const u64 want = T64 & 0xffffffffffffull;
      for (unsigned sp = 0; sp < GSPIN; ++sp) {
        u64 v[8]; bool ok = true;
#pragma unroll
        for (int i = 0; i < 8; ++i) v[i] = __hip_atomic_load(qg + i * 64, RLX_AGENT);
#pragma unroll
        for (int i = 0; i < 8; ++i) { ok &= (v[i] >> 16) == want; ((LAS unsigned short*)(L + L_Q))[i * 64 + lane] = (unsigned short)v[i]; }
        if (__all(ok)) break;
        __builtin_amdgcn_s_sleep(2);
      }
      LDS_WAIT(); CT[C_QREADY] = 1u;
    }
  }
  const unsigned T32 = (unsigned)T64;
  lds_wait_ge(CT + C_QREADY, 1u);
  v4u qf[4];
#pragma unroll
  for (int ks = 0; ks < 4; ++ks) qf[ks] = *(const LAS v4u*)(L + L_Q + ((r & 3) * 128 + 32 * ks + 8 * g) * 2);

  f32x4 acc[8], accl = {0.f, 0.f, 0.f, 0.f};
#pragma unroll
  for (int m = 0; m < 8; ++m) acc[m] = f32x4{0.f, 0.f, 0.f, 0.f};
  float mrun = -INFINITY;
  const v4u ones = {0x3F803F80u, 0x3F803F80u, 0x3F803F80u, 0x3F803F80u};
  LAS unsigned char* kbase = L + L_RING + c * SLOT + r * 256;
  LAS unsigned char* vbase = L + L_RING + (4 + c) * SLOT + (4 * g + (r >> 2)) * 256 + (r & 3) * 8;
  const int vx = 4 * (g & 1) + (r >> 2);
  for (int lap = 0; lap < NLAP; ++lap) {
    lds_wait_ge(CT + C_LANDED, (unsigned)(3 + lap * 8 + c));
    v4u kf[4][4];
#pragma unroll
    for (int T = 0; T < 4; ++T)
#pragma unroll
      for (int ks = 0; ks < 4; ++ks) kf[T][ks] = *(const LAS v4u*)(kbase + T * 4096 + (((4 * ks + g) ^ r) * 16));
    LDS_WAIT();
    CT[C_DONE + c] = (unsigned)(2 * lap + 1);
#pragma unroll
    for (int T = 0; T < 4; ++T) asm volatile("" : "+v"(kf[T][0]), "+v"(kf[T][1]), "+v"(kf[T][2]), "+v"(kf[T][3]) :: "memory");
    f32x4 sc[4];
#pragma unroll
    for (int T = 0; T < 4; ++T) {
      f32x4 d = {0.f, 0.f, 0.f, 0.f};
#pragma unroll
      for (int ks = 0; ks < 4; ++ks) d = mfma(kf[T][ks], qf[ks], d);
      sc[T] = d;
    }
    float tm = fmaxf(fmaxf(sc[0][0], sc[0][1]), fmaxf(sc[0][2], sc[0][3]));
#pragma unroll
    for (int T = 1; T < 4; ++T) tm = fmaxf(tm, fmaxf(fmaxf(sc[T][0], sc[T][1]), fmaxf(sc[T][2], sc[T][3])));
    tm = xrow_max(tm);
    if (__any(tm > mrun)) {
      const float mn = fmaxf(mrun, tm);
      const float al = __builtin_amdgcn_exp2f((mrun - mn) * SC2);
#pragma unroll
      for (int m = 0; m < 8; ++m) acc[m] *= al;
      accl *= al;
      mrun = mn;
    }
    const float mo = -mrun * SC2;
    v4u pf[2];
#pragma unroll
    for (int T = 0; T < 4; ++T) {
      float p[4];
#pragma unroll
      for (int e = 0; e < 4; ++e) p[e] = __builtin_amdgcn_exp2f(fmaf(sc[T][e], SC2, mo));
      pf[T >> 1][(T & 1) * 2] = cvtpk(p[0], p[1]);
      pf[T >> 1][(T & 1) * 2 + 1] = cvtpk(p[2], p[3]);
    }
    if (lap == 1 && c == 0) {
      gu64* fl = ws64 + W_FLAG / 8 + lane;
      for (unsigned sp = 0; sp < GSPIN; ++sp) {
        bool ok = true;
#pragma unroll
        for (int i = 0; i < 4; ++i) ok &= __hip_atomic_load(fl + i * 64, RLX_AGENT) == T64;
        if (__all(ok)) break;
        __builtin_amdgcn_s_sleep(8);
      }
      CT[C_CLEAN] = 1u;
    }
    lds_wait_ge(CT + C_LANDED, (unsigned)(7 + lap * 8 + c));
    v4u vf[2][8];
#pragma unroll
    for (int ks = 0; ks < 2; ++ks)
#pragma unroll
      for (int m = 0; m < 8; ++m) {
        LAS unsigned char* p = vbase + ks * 8192 + ((m ^ vx) * 32);
        const v2u t0 = trread(p), t1 = trread(p + 4096);
        vf[ks][m] = v4u{t0.x, t0.y, t1.x, t1.y};
      }
    LDS_WAIT();
    CT[C_DONE + c] = (unsigned)(2 * lap + 2);
#pragma unroll
    for (int ks = 0; ks < 2; ++ks) {
      asm volatile("" : "+v"(vf[ks][0]), "+v"(vf[ks][1]), "+v"(vf[ks][2]), "+v"(vf[ks][3]) :: "memory");
      asm volatile("" : "+v"(vf[ks][4]), "+v"(vf[ks][5]), "+v"(vf[ks][6]), "+v"(vf[ks][7]) :: "memory");
    }
#pragma unroll
    for (int ks = 0; ks < 2; ++ks) {
#pragma unroll
      for (int m = 0; m < 8; ++m) acc[m] = mfma(vf[ks][m], pf[ks], acc[m]);
      accl = mfma(ones, pf[ks], accl);
    }
  }

  {
    LAS float* rec = (LAS float*)(L + L_RING + c * SLOT);
    if (r < 4) {
#pragma unroll
      for (int m = 0; m < 8; ++m) *(LAS f32x4*)(rec + r * 128 + 16 * m + 4 * g) = acc[m];
      if (g == 0) { rec[512 + r] = mrun; rec[516 + r] = accl[0]; }
    }
    LDS_WAIT();
    CT[C_MRG + c] = 1u;
#pragma unroll
    for (int w = 0; w < 4; ++w) lds_wait_ge(CT + C_MRG + w, 1u);
    float mw[4], lw[4]; f32x2 ow[4];
#pragma unroll
    for (int w = 0; w < 4; ++w) {
      const LAS float* rw = (const LAS float*)(L + L_RING + w * SLOT);
      mw[w] = rw[512 + c]; lw[w] = rw[516 + c]; ow[w] = *(const LAS f32x2*)(rw + c * 128 + 2 * lane);
    }
    const float M = fmaxf(fmaxf(mw[0], mw[1]), fmaxf(mw[2], mw[3]));
    float Ls = 0.f, o0 = 0.f, o1 = 0.f;
#pragma unroll
    for (int w = 0; w < 4; ++w) {
      const float f = __builtin_amdgcn_exp2f((mw[w] - M) * SC2);
      Ls = fmaf(f, lw[w], Ls); o0 = fmaf(f, ow[w][0], o0); o1 = fmaf(f, ow[w][1], o1);
    }
    const float inv = 1.f / Ls;
    gu64* pg = ws64 + W_PG / 8 + ((size_t)bid * 4 + c) * PGH;
    __hip_atomic_store(pg + lane, ((u64)T32 << 32) | cvtpk(o0 * inv, o1 * inv), RLX_AGENT);
    if (lane < 2) __hip_atomic_store(pg + 64 + lane, ((u64)T32 << 32) | __float_as_uint(lane == 0 ? M * SC2 : Ls), RLX_AGENT);
  }

  lds_wait_ge(CT + C_CLEAN, 1u);
  {
    v4u af[2]; float Ms[2], Lq[2];
    v4u hf = {0u, 0u, 0u, 0u};
    if (c < 2) {
      const bool live = (r & 3) < 2 && (c == 0 || g < 3);
      const int hb = live ? 4 * (r & 3) + (r >> 2) : 0;
      gu64* hp = (gu64*)(a.ws + W_H + ((size_t)hb * HP + ht * 56 + 32 * c + 8 * (live ? g : 0)) * 2);
      const u64 h0 = __hip_atomic_load(hp, RLX_AGENT), h1 = __hip_atomic_load(hp + 1, RLX_AGENT);
      if (live) hf = v4u{(unsigned)h0, (unsigned)(h0 >> 32), (unsigned)h1, (unsigned)(h1 >> 32)};
    }
    gu64* pa = ws64 + W_PG / 8 + ((size_t)(kap * 32 + r) * 4 + hh) * PGH + 16 * c + 4 * g;
    gu64* pm = ws64 + W_PG / 8 + ((size_t)(kap * 32 + 4 * g + (r & 3)) * 4 + hh) * PGH + 64;
    for (unsigned sp = 0; sp < GSPIN; ++sp) {
      u64 v[2][4], mv[2][2]; bool ok = true;
#pragma unroll
      for (int rt = 0; rt < 2; ++rt) {
#pragma unroll
        for (int e = 0; e < 4; ++e) v[rt][e] = __hip_atomic_load(pa + (size_t)rt * 16 * 4 * PGH + e, RLX_AGENT);
#pragma unroll
        for (int e = 0; e < 2; ++e) mv[rt][e] = __hip_atomic_load(pm + (size_t)rt * 16 * 4 * PGH + e, RLX_AGENT);
      }
#pragma unroll
      for (int rt = 0; rt < 2; ++rt) {
#pragma unroll
        for (int e = 0; e < 4; ++e) { ok &= (unsigned)(v[rt][e] >> 32) == T32; af[rt][e] = (unsigned)v[rt][e]; }
        ok &= (unsigned)(mv[rt][0] >> 32) == T32 && (unsigned)(mv[rt][1] >> 32) == T32;
        Ms[rt] = __uint_as_float((unsigned)mv[rt][0]); Lq[rt] = __uint_as_float((unsigned)mv[rt][1]);
      }
      if (__all(ok)) break;
      __builtin_amdgcn_s_sleep(1);
    }
    lds_wait_ge(CT + C_LANDED, 68u);
    float res[2][4];
    LAS unsigned char* wb = L + L_A + (32 * c + 8 * g + (r >> 2)) * 128 + (r & 3) * 8;
    v4u bw[4];
#pragma unroll
    for (int ct = 0; ct < 4; ++ct) { const v2u t0 = trread(wb + ct * 32), t1 = trread(wb + ct * 32 + 512); bw[ct] = v4u{t0.x, t0.y, t1.x, t1.y}; }
#pragma unroll
    for (int rt = 0; rt < 2; ++rt) {
      float Mx = fmaxf(Ms[rt], dpp<0xB1>(Ms[rt])); Mx = fmaxf(Mx, dpp<0x4E>(Mx));
      const float wl = __builtin_amdgcn_exp2f(Ms[rt] - Mx) * Lq[rt];
      float Lt = wl + dpp<0xB1>(wl); Lt += dpp<0x4E>(Lt);
      const float gate = ((volatile LAS float*)(CT + C_G))[4 * rt + g];
      const float wn = wl * (gate / Lt);
      const float w0 = dpp<0x00>(wn), w1 = dpp<0x55>(wn), w2 = dpp<0xAA>(wn), w3 = dpp<0xFF>(wn);
#pragma unroll
      for (int ct = 0; ct < 4; ++ct) {
        const f32x4 d = mfma(af[rt], bw[ct], f32x4{0.f, 0.f, 0.f, 0.f});
        res[rt][ct] = (w0 * d[0] + w1 * d[1]) + (w2 * d[2] + w3 * d[3]);
      }
    }
    if (c < 2) {
      LAS unsigned char* db = L + L_B + (32 * c + 8 * g + (r >> 2)) * 128 + (r & 3) * 8;
      const float g0 = 1.f - ((volatile LAS float*)(CT + C_G))[g], g1 = 1.f - ((volatile LAS float*)(CT + C_G))[4 + g];
#pragma unroll
      for (int ct = 0; ct < 4; ++ct) {
        const v2u t0 = trread(db + ct * 32), t1 = trread(db + ct * 32 + 512);
        const f32x4 d = mfma(hf, v4u{t0.x, t0.y, t1.x, t1.y}, f32x4{0.f, 0.f, 0.f, 0.f});
        res[0][ct] = fmaf(g0, d[0], res[0][ct]); res[1][ct] = fmaf(g1, d[1], res[1][ct]);
      }
    }
    LAS float* red = (LAS float*)(L + L_RING + 4 * SLOT);
#pragma unroll
    for (int rt = 0; rt < 2; ++rt)
#pragma unroll
      for (int ct = 0; ct < 4; ++ct) red[((c * 2 + rt) * 4 + ct) * 64 + lane] = res[rt][ct];
    LDS_WAIT();
    CT[C_RED + c] = 1u;
#pragma unroll
    for (int w = 0; w < 4; ++w) lds_wait_ge(CT + C_RED + w, 1u);
#pragma unroll
    for (int rt = 0; rt < 2; ++rt) {
      float t = 0.f;
#pragma unroll
      for (int w = 0; w < 4; ++w) t += red[((w * 2 + rt) * 4 + c) * 64 + lane];
      gu64* og = ws64 + W_OG / 8 + ((size_t)ht * NB + 4 * rt + g) * DM + n8 * 64 + 16 * c + r;
      __hip_atomic_store(og, ((u64)T32 << 32) | __float_as_uint(t), RLX_AGENT);
    }
  }

  if (c == 0) {
    const int bf = bid >> 5, n32 = bid & 31;
    gu64* og = ws64 + W_OG / 8 + ((size_t)(8 * g) * NB + bf) * DM + n32 * 16 + r;
    float t = 0.f;
    for (unsigned sp = 0; sp < GSPIN; ++sp) {
      u64 v[8]; bool ok = true;
#pragma unroll
      for (int i = 0; i < 8; ++i) v[i] = __hip_atomic_load(og + (size_t)i * NB * DM, RLX_AGENT);
      t = 0.f;
#pragma unroll
      for (int i = 0; i < 8; ++i) { ok &= (unsigned)(v[i] >> 32) == T32; t += __uint_as_float((unsigned)v[i]); }
      if (__all(ok)) break;
      __builtin_amdgcn_s_sleep(1);
    }
    t = xrow_sum(t);
    if (g == 0) ((GAS unsigned short*)a.out)[bf * DM + n32 * 16 + r] = (unsigned short)(cvtpk(t, 0.f) & 0xffffu);
    if (bid == 0 && lane == 0) __hip_atomic_store(ws64 + W_EPOCH / 8, T64, RLX_AGENT);
  }
}
}

extern "C" void kernel_launch(void* const* d_in, const int* in_sizes, int n_in, void* d_out, int out_size, void* d_ws,
                              size_t ws_size, hipStream_t stream) {
  (void)in_sizes; (void)n_in; (void)out_size;
  if (ws_size < W_END) return;
  Args a;
  a.x = (const unsigned char*)d_in[0]; a.k = (const unsigned char*)d_in[1]; a.v = (const unsigned char*)d_in[2];
  a.wq = (const unsigned char*)d_in[3]; a.wo = (const unsigned char*)d_in[4];
  a.wg = (const unsigned char*)d_in[6]; a.wu = (const unsigned char*)d_in[7]; a.wd = (const unsigned char*)d_in[8];
  a.gw = (const unsigned char*)d_in[9];
  a.out = (unsigned char*)d_out; a.ws = (unsigned char*)d_ws;
  (void)hipFuncSetAttribute((const void*)fused_kernel, hipFuncAttributeMaxDynamicSharedMemorySize, L_BYTES);
  hipLaunchKernelGGL(fused_kernel, dim3(NWG), dim3(NTHR), L_BYTES, stream, a);
}
```

```cpp
#define DIAG_TS
#define DIAG_PRINT
#include <hip/hip_runtime.h>
#include <hip/hip_bf16.h>
#include <stdint.h>

#define GAS __attribute__((address_space(1)))
#define LAS __attribute__((address_space(3)))
typedef unsigned v4u __attribute__((ext_vector_type(4)));
typedef unsigned v2u __attribute__((ext_vector_type(2)));
typedef float f32x4 __attribute__((ext_vector_type(4)));
typedef float f32x2 __attribute__((ext_vector_type(2)));
typedef __bf16 bf16x8 __attribute__((ext_vector_type(8)));
typedef short s16x4 __attribute__((ext_vector_type(4)));
typedef unsigned long long u64;
typedef GAS u64 gu64;
#define RLX_AGENT __ATOMIC_RELAXED, __HIP_MEMORY_SCOPE_AGENT

namespace {
constexpr int NB = 8, DM = 512, HQ = 32, HKV = 8, SK = 8192, DH = 128, FF = 1792;
constexpr int NWG = 256, NTHR = 6 * 64;
constexpr int NTILE = 32;
#define QG_AT 6u
#define H1_SLEEP 6
#define H2_SLEEP 6
#define ML_SLEEP 8
constexpr int SLOT = 16384;
constexpr int L_RING = 0, L_A = 8 * SLOT, L_B = L_A + SLOT, L_Q = L_B + 8192, L_HB = L_Q + 1024, L_ML = L_HB + 2048, L_CTRL = L_ML + 256, L_BYTES = L_CTRL + 256;
constexpr int STG_ROW = 272;
constexpr int HB_ROW = 144;
constexpr int C_LANDED = 0, C_GUFREE = 1, C_QRDY = 2, C_SEEN = 3, C_DONE = 4, C_MRG = 8, C_STG = 12, C_TAG = 16, C_TAGOK = 18, C_WOK = 19, C_CLEAN = 20, C_G = 24;
static_assert(L_BYTES <= 160 * 1024, "LDS");
constexpr size_t W_EPOCH = 0, W_G = 256, W_FLAG = 4096;
constexpr size_t W_QG = 8192;
constexpr size_t W_GP = W_QG + (size_t)2 * NB * 4096 * 8;
constexpr size_t W_HG = W_GP + (size_t)224 * 2048;
constexpr size_t W_PG = W_HG + (size_t)225 * 512 + 512;
constexpr unsigned PGB = 16384 + 512;
constexpr size_t W_OG = W_PG + (size_t)HQ * PGB;
constexpr size_t W_END = W_OG + (size_t)NWG * 4096;
static_assert(W_END < (1u << 30) && W_PG % 1024 == 0, "buffer offsets are int");
constexpr u64 EPOCH_STEP = 0x9E3779B97F4A7C15ull;
constexpr unsigned GSPIN = 1u << 15, LSPIN = 1u << 18;
constexpr float SC2 = 0.08838834764831845f * 1.4426950408889634f;

struct Args {
  const unsigned char *x, *k, *v, *wq, *wo, *wg, *wu, *wd, *gw;
  unsigned char *out, *ws;
};

__device__ __forceinline__ f32x4 mfma(v4u a, v4u b, f32x4 c) {
  return __builtin_amdgcn_mfma_f32_16x16x32_bf16(__builtin_bit_cast(bf16x8, a), __builtin_bit_cast(bf16x8, b), c, 0, 0, 0);
}
__device__ __forceinline__ v2u trread(LAS unsigned char* p) {
  return __builtin_bit_cast(v2u, __builtin_amdgcn_ds_read_tr16_b64_v4i16((LAS s16x4*)p));
}
__device__ __forceinline__ unsigned cvtpk(float lo, float hi) {
  typedef __bf16 bf16x2 __attribute__((ext_vector_type(2)));
  bf16x2 r; r[0] = (__bf16)lo; r[1] = (__bf16)hi; return __builtin_bit_cast(unsigned, r);
}
template <int CTRL> __device__ __forceinline__ float dpp(float x) {
  return __builtin_bit_cast(float, __builtin_amdgcn_mov_dpp(__builtin_bit_cast(int, x), CTRL, 0xf, 0xf, true));
}
__device__ __forceinline__ float xrow_max(float x) {
  auto s = __builtin_amdgcn_permlane16_swap(__float_as_uint(x), __float_as_uint(x), false, false);
  x = fmaxf(__uint_as_float(s[0]), __uint_as_float(s[1]));
  auto t = __builtin_amdgcn_permlane32_swap(__float_as_uint(x), __float_as_uint(x), false, false);
  return fmaxf(__uint_as_float(t[0]), __uint_as_float(t[1]));
}
__device__ __forceinline__ float xrow_sum(float x) {
  auto s = __builtin_amdgcn_permlane16_swap(__float_as_uint(x), __float_as_uint(x), false, false);
  x = __uint_as_float(s[0]) + __uint_as_float(s[1]);
  auto t = __builtin_amdgcn_permlane32_swap(__float_as_uint(x), __float_as_uint(x), false, false);
  return __uint_as_float(t[0]) + __uint_as_float(t[1]);
}
__device__ __forceinline__ float bflo(unsigned u) { return __uint_as_float(u << 16); }
__device__ __forceinline__ float bfhi(unsigned u) { return __uint_as_float(u & 0xffff0000u); }

__device__ __forceinline__ v4u ld16(__amdgpu_buffer_rsrc_t rs, unsigned off) { return __builtin_amdgcn_raw_buffer_load_b128(rs, (int)off, 0, 16); }
__device__ __forceinline__ void st16(__amdgpu_buffer_rsrc_t rs, unsigned off, v4u v) { __builtin_amdgcn_raw_buffer_store_b128(v, rs, (int)off, 0, 16); }
__device__ __forceinline__ unsigned f24(float v) { return (__float_as_uint(v) + 0x80u) >> 8; }
#define TAG_LO ((unsigned)T64 << 24)
#define TAG_HI ((unsigned)(T64 >> 8))
#define TAG_OK(lo, hi) ((hi) == TAG_HI && ((lo) ^ TAG_LO) < 0x1000000u)
#define CBAR() asm volatile("" ::: "memory")
#define LDS_WAIT() asm volatile("s_waitcnt lgkmcnt(0)" ::: "memory")
#define KPOL " nt"
#define VPOL " nt"
#define WPOL " nt"
#define DMA4(sbase, dst, v0, v1, v2, v3, POL)                                                                    \
  asm volatile("s_mov_b32 m0, %5\n\ts_nop 0\n\t"                                                                 \
               "global_load_lds_dwordx4 %0, %4" POL "\n\t"                                                       \
               "global_load_lds_dwordx4 %1, %4 offset:1024" POL "\n\t"                                           \
               "global_load_lds_dwordx4 %2, %4 offset:2048" POL "\n\t"                                           \
               "global_load_lds_dwordx4 %3, %4 offset:3072" POL                                                  \
               :: "v"(v0), "v"(v1), "v"(v2), "v"(v3), "s"(sbase), "s"(dst) : "memory")
#define DMA1S(sbase, voff, dst)                                                                                  \
  asm volatile("s_mov_b32 m0, %2\n\ts_nop 0\n\tglobal_load_lds_dwordx4 %0, %1" WPOL :: "v"(voff), "s"(sbase), "s"(dst) : "memory")
#define DMA1V(vaddr, dst)                                                                                        \
  asm volatile("s_mov_b32 m0, %1\n\ts_nop 0\n\tglobal_load_lds_dwordx4 %0, off" WPOL :: "v"(vaddr), "s"(dst) : "memory")
#define VMWAIT(n) asm volatile("s_waitcnt vmcnt(" #n ")" ::: "memory")
#define VMWAITN(n) asm volatile("s_waitcnt vmcnt(%0)" :: "i"(n) : "memory")
#define TS(i) do { tsv[(i)] = __builtin_amdgcn_s_memtime(); } while (0)
#define TS_FLUSH(w) do { const u64 c1_ = __builtin_amdgcn_s_memtime(), r1_ = __builtin_amdgcn_s_memrealtime(); if (lane == 0) { GAS u64* t_ = (GAS u64*)(a.ws + W_END) + ((size_t)bid * 6 + (w)) * 12; \
    t_[0] = tsv[0]; t_[1] = tsv[1]; t_[2] = tsv[2]; t_[3] = tsv[3]; t_[4] = ts_c0; t_[5] = ts_r0; t_[6] = c1_; t_[7] = r1_; t_[8] = tsv[4]; t_[9] = tsv[5]; t_[10] = tsv[6]; t_[11] = tsv[7]; } } while (0)

__device__ __forceinline__ void lds_wait_ge(volatile LAS unsigned* w, unsigned need) {
#pragma unroll 1
  for (unsigned sp = 0; *w < need && sp < LSPIN; ++sp) __builtin_amdgcn_s_sleep(1);
  CBAR();
}

__global__ void __launch_bounds__(NTHR, 2) fused_kernel(Args a) {
  extern __shared__ __attribute__((aligned(16))) unsigned char smem[];
  LAS unsigned char* L = (LAS unsigned char*)smem;
  volatile LAS unsigned* CT = (volatile LAS unsigned*)(L + L_CTRL);
  const int tid = threadIdx.x, lane = tid & 63, wave = __builtin_amdgcn_readfirstlane(tid >> 6);
  const int bid = blockIdx.x;
  const int kap = bid >> 5, jg = bid & 31, b = jg >> 2, s = jg & 3;
  const int qcol = kap * 512 + (((jg & 7) << 1) | ((jg >> 3) & 1)) * 32, qk = jg >> 4;
  const bool has_ffn = (bid >> 3) < 28;
  const int fi = has_ffn ? (bid >> 3) : 27;
  const int fcg = (bid & 7) * 7 + (fi >> 2), fk = fi & 3;
  const int hh = jg >> 3, ht = kap * 4 + hh, n8 = jg & 7;
  const int g = lane >> 4, r = lane & 15;
  gu64* ws64 = (gu64*)a.ws;
  const __amdgpu_buffer_rsrc_t rs = __builtin_amdgcn_make_buffer_rsrc((void*)a.ws, 0, (int)W_END, 0x00020000);
  u64 tsv[8] = {0, 0, 0, 0, 0, 0, 0, 0};
  const u64 ts_c0 = __builtin_amdgcn_s_memtime(), ts_r0 = __builtin_amdgcn_s_memrealtime();
  if (wave == 0) {
    __builtin_amdgcn_s_waitcnt(0);
    __builtin_amdgcn_s_barrier();
    CBAR();
    __builtin_amdgcn_s_setprio(3);
    const unsigned lb = (unsigned)(size_t)L;
    {
      const unsigned voff = (unsigned)(lane >> 2) * 8192u + (unsigned)(lane & 3) * 16u;
      const unsigned char* sb = a.wq + (size_t)qk * 256 * 8192 + (size_t)qcol * 2;
#pragma unroll 1
      for (int j = 0; j < 16; ++j) DMA1S(sb + (size_t)j * 16 * 8192, voff, lb + L_A + j * 1024);
    }
    {
      const unsigned char* p = ((lane & 32) ? a.wu : a.wg) + (size_t)(fk * 128 + ((lane >> 2) & 7)) * (FF * 2) + (size_t)fcg * 64 + (lane & 3) * 16;
#pragma unroll 1
      for (int j = 0; j < 16; ++j) DMA1V(p + (size_t)j * 8 * (FF * 2), lb + L_RING + 7 * SLOT + j * 1024);
    }
    unsigned ko[4], vo[2];
#pragma unroll
    for (int j = 0; j < 4; ++j) ko[j] = (unsigned)g * 256u + (unsigned)((r ^ g ^ (4 * j)) * 16);
#pragma unroll
    for (int j = 0; j < 2; ++j) vo[j] = (unsigned)g * 256u + (unsigned)((r ^ (2 * (4 * j + g))) * 16);
    const size_t seq_off = (size_t)(b * HKV + kap) * SK * 256;
    const unsigned char* kb = a.k + seq_off;
    const unsigned char* vb = a.v + seq_off;
    unsigned seen = 0;
#pragma unroll 1
    for (int n = 0; n < NTILE; ++n) {
      const int tile = ((n + 4 * b) & 31) * 4 + s;
      const int cs = n & 3;
#pragma unroll
      for (int hv = 0; hv < 2; ++hv) {
        const unsigned f = 2u + 2u * n + hv;
        if (n >= 4 || (n == 3 && hv == 1)) {
          volatile LAS unsigned* fw = n >= 4 ? (CT + C_DONE + cs) : (CT + C_GUFREE);
          const unsigned need = n >= 4 ? (unsigned)(2 * (n >> 2) - 1 + hv) : 1u;
          if (n < 4 || seen < need) {
            if (*fw < need) {
              VMWAIT(0); CT[C_LANDED] = f;
              lds_wait_ge(fw, need);
            }
          }
        }
        const unsigned char* sb = (hv ? vb : kb) + (size_t)tile * SLOT;
        const unsigned dst = lb + L_RING + (hv * 4 + cs) * SLOT;
        if (hv == 0) { DMA4(sb, dst, ko[0], ko[1], ko[2], ko[3], KPOL); DMA4(sb + 4096, dst + 4096, ko[0], ko[1], ko[2], ko[3], KPOL); }
        else         { DMA4(sb, dst, vo[0], vo[1], vo[0], vo[1], VPOL); DMA4(sb + 4096, dst + 4096, vo[0], vo[1], vo[0], vo[1], VPOL); }
        if (n) VMWAIT(16);
        if (hv == 0) { DMA4(sb + 8192, dst + 8192, ko[0], ko[1], ko[2], ko[3], KPOL); DMA4(sb + 12288, dst + 12288, ko[0], ko[1], ko[2], ko[3], KPOL); }
        else         { DMA4(sb + 8192, dst + 8192, vo[0], vo[1], vo[0], vo[1], VPOL); DMA4(sb + 12288, dst + 12288, vo[0], vo[1], vo[0], vo[1], VPOL); }
        const unsigned nx = CT[C_DONE + (hv ? ((cs + 1) & 3) : cs)];
        if (n == 0) { VMWAIT(32); CT[C_LANDED] = f - 1; }
        else        { VMWAIT(16); CT[C_LANDED] = f; }
        seen = __builtin_amdgcn_readfirstlane(nx);
        if (f == 12) TS(1);
        if (f == 3) TS(5);
        if (f == 6) TS(6);
        if (f == 34) TS(7);
      }
    }
    {
      const unsigned voff = (unsigned)(lane >> 3) * 1024u + (unsigned)(lane & 7) * 16u;
      const unsigned char* sb = a.wo + (size_t)ht * 128 * 1024 + (size_t)n8 * 128;
      const unsigned char* sd = a.wd + (size_t)ht * 56 * 1024 + (size_t)n8 * 128;
#pragma unroll 1
      for (int j = 0; j < 23; ++j) DMA1S(j < 16 ? sb + (size_t)j * 8192 : sd + (size_t)(j - 16) * 8192, voff, lb + L_A + j * 1024);
      VMWAIT(23); CT[C_LANDED] = 2u + 2u * NTILE;
      TS(2);
      VMWAIT(0); CT[C_WOK] = 1u;
      TS(3);
      TS_FLUSH(0);
    }
    return;
  }

  const int c = wave - 1;
  u64 T64 = 0;
  v4u xf[8];
  if (wave == 5) T64 = __hip_atomic_load(ws64 + W_EPOCH / 8, RLX_AGENT);
  if (wave <= 2) {
    const GAS v4u* xp = (const GAS v4u*)(a.x + (size_t)(r & 7) * 1024 + (wave == 1 ? qk * 512 : fk * 256) + g * 16);
#pragma unroll
    for (int ks = 0; ks < 8; ++ks) xf[ks] = xp[(wave == 1 ? ks : (ks & 3)) * 4];
  }
  if (wave == 3) ((LAS unsigned*)(L + L_CTRL))[lane] = 0u;
  if (wave == 4) ((LAS v4u*)(L + L_B + 7168))[lane] = v4u{0u, 0u, 0u, 0u};
  LDS_WAIT();
  __builtin_amdgcn_s_barrier();
  CBAR();

  if (wave == 5) {
    T64 += EPOCH_STEP;
    if (lane == 0) { CT[C_TAG] = (unsigned)T64; CT[C_TAG + 1] = (unsigned)(T64 >> 32); LDS_WAIT(); CT[C_TAGOK] = 1u; }
    if (bid == NWG - 1) {
      const int gb = lane >> 3, part = lane & 7;
      const GAS v4u* xp = (const GAS v4u*)(a.x + (size_t)gb * 1024 + part * 128);
      const GAS v4u* gp = (const GAS v4u*)(a.gw + part * 128);
      float ga = 0.f;
#pragma unroll
      for (int i = 0; i < 8; ++i) {
        const v4u xv = xp[i], gv = gp[i];
#pragma unroll
        for (int e = 0; e < 4; ++e) { ga = fmaf(bflo(xv[e]), bflo(gv[e]), ga); ga = fmaf(bfhi(xv[e]), bfhi(gv[e]), ga); }
      }
      ga += dpp<0xB1>(ga); ga += dpp<0x4E>(ga); ga += dpp<0x141>(ga);
      const float gate = 1.f / (1.f + __expf(-ga));
      if (part == 0) __hip_atomic_store(ws64 + W_G / 8 + gb, ((u64)TAG_HI << 32) | (f24(gate) | TAG_LO), RLX_AGENT);
    }
    {
      lds_wait_ge(CT + C_LANDED, QG_AT);
      const unsigned qo = (unsigned)W_QG + (unsigned)((b * HKV + kap) * 8192 + lane * 16);
      _Pragma("unroll 1") for (unsigned sp = 0; sp < GSPIN; ++sp) {
        v4u v[8]; bool ok = true;
#pragma unroll
        for (int i = 0; i < 8; ++i) v[i] = ld16(rs, qo + i * 1024u);
#pragma unroll
        for (int i = 0; i < 4; ++i) {
          ok &= TAG_OK(v[i].x, v[i].y) && TAG_OK(v[i].z, v[i].w) && TAG_OK(v[i + 4].x, v[i + 4].y) && TAG_OK(v[i + 4].z, v[i + 4].w);
          ((LAS unsigned*)(L + L_Q))[i * 64 + lane] = cvtpk(__uint_as_float(v[i].x << 8) + __uint_as_float(v[i + 4].x << 8), __uint_as_float(v[i].z << 8) + __uint_as_float(v[i + 4].z << 8));
        }
        if (__all(ok)) break;
        __builtin_amdgcn_s_sleep(8);
      }
      LDS_WAIT(); CT[C_QRDY] = 1u;
      TS(2);
    }
    {
      lds_wait_ge(CT + C_LANDED, 12u);
      const int fb = lane < 32 ? kap * 32 + lane : (bid & 7) + 8 * ((fi & ~3) + (lane & 3));
      _Pragma("unroll 1") for (unsigned sp = 0; sp < GSPIN; ++sp) {
        if (__all(__hip_atomic_load(ws64 + W_FLAG / 8 + fb, RLX_AGENT) == T64)) break;
        __builtin_amdgcn_s_sleep(32);
      }
      CBAR();
    }
    if (has_ffn) {
      const unsigned go = (unsigned)W_GP + (unsigned)((fcg * 4 + fk) * 2048 + lane * 16);
      const f32x4 p0 = __builtin_bit_cast(f32x4, ld16(rs, go)), p1 = __builtin_bit_cast(f32x4, ld16(rs, go + 1024u));
      unsigned hw[4];
#pragma unroll
      for (int e = 0; e < 4; ++e) {
        const unsigned t = __float_as_uint(p0[e] + p1[e]);
        auto s32 = __builtin_amdgcn_permlane32_swap(t, t, false, false);
        const unsigned tot = __float_as_uint(__uint_as_float(s32[0]) + __uint_as_float(s32[1]));
        auto s16 = __builtin_amdgcn_permlane16_swap(tot, tot, false, false);
        const float gt = __uint_as_float(s16[0]), up = __uint_as_float(s16[1]);
        const float u = 0.7978845608028654f * (gt + 0.044715f * gt * gt * gt);
        const float th = 1.f - 2.f / (__expf(2.f * u) + 1.f);
        hw[e] = f24(0.5f * gt * (1.f + th) * up) | TAG_LO;
      }
      if (lane < 16) {
        const unsigned ho = (unsigned)W_HG + (unsigned)((fcg * 4 + fk) * 512 + lane * 32);
        st16(rs, ho, v4u{hw[0], TAG_HI, hw[1], TAG_HI});
        st16(rs, ho + 16u, v4u{hw[2], TAG_HI, hw[3], TAG_HI});
      }
    }
    {
      lds_wait_ge(CT + C_LANDED, 20u);
      const unsigned ho = (unsigned)W_HG + (unsigned)(ht * 7 * 512 + lane * 16);
      _Pragma("unroll 1") for (unsigned sp = 0; sp < GSPIN; ++sp) {
        v4u v[4];
#pragma unroll
        for (int i = 0; i < 4; ++i) v[i] = ld16(rs, ho + i * 1024u);
        const u64 gv = __hip_atomic_load(ws64 + W_G / 8 + (lane & 7), RLX_AGENT);
        bool ok = TAG_OK((unsigned)gv, (unsigned)(gv >> 32));
#pragma unroll
        for (int i = 0; i < 4; ++i) {
          const bool pad = i == 3 && lane >= 32;
          ok &= pad || (TAG_OK(v[i].x, v[i].y) && TAG_OK(v[i].z, v[i].w));
          *(LAS unsigned*)(L + L_HB + ((lane >> 2) & 7) * HB_ROW + (2 * i + (lane >> 5)) * 16 + (lane & 3) * 4) =
              pad ? 0u : cvtpk(__uint_as_float(v[i].x << 8), __uint_as_float(v[i].z << 8));
        }
        if (lane < 8) CT[C_G + lane] = (unsigned)gv << 8;
        if (__all(ok)) break;
        __builtin_amdgcn_s_sleep(32);
      }
      LDS_WAIT();
      CT[C_CLEAN] = 1u;
      TS(1);
    }
    {
      lds_wait_ge(CT + C_LANDED, 2u * NTILE - 2u);
      gu64* mlp = (gu64*)(a.ws + W_PG + (size_t)ht * PGB + 16384) + lane;
      u64 ma = __hip_atomic_load(mlp, RLX_AGENT), mb;
#define ML_CHECK(v) do { const bool mk = TAG_OK((unsigned)v, (unsigned)(v >> 32)); if (mk) ((LAS unsigned*)(L + L_ML))[lane] = (unsigned)v << 8; \
        u64 k = __ballot(mk); k &= k >> 1; k &= 0x5555555555555555ull; \
        k = (k | (k >> 1)) & 0x3333333333333333ull; k = (k | (k >> 2)) & 0x0f0f0f0f0f0f0f0full; k = (k | (k >> 4)) & 0x00ff00ff00ff00ffull; \
        k = (k | (k >> 8)) & 0x0000ffff0000ffffull; k = (k | (k >> 16)) & 0xffffffffull; \
        sn = (unsigned)k; LDS_WAIT(); CT[C_SEEN] = sn; } while (0)
      unsigned sn;
      _Pragma("unroll 1") for (unsigned sp = 0; sp < GSPIN; ++sp) {
        __builtin_amdgcn_s_sleep(ML_SLEEP);
        mb = __hip_atomic_load(mlp, RLX_AGENT);
        ML_CHECK(ma);
        if (sn == ~0u) break;
        __builtin_amdgcn_s_sleep(ML_SLEEP);
        ma = __hip_atomic_load(mlp, RLX_AGENT);
        ML_CHECK(mb);
        if (sn == ~0u) break;
      }
    }
    TS_FLUSH(5);
    return;
  }

  if (c == 0) {
    lds_wait_ge(CT + C_TAGOK, 1u);
    T64 = ((u64)CT[C_TAG + 1] << 32) | CT[C_TAG];
    lds_wait_ge(CT + C_LANDED, 1u);
    TS(0);
    LAS unsigned char* sl = L + L_A + (8 * g + (r >> 2)) * 64 + (r & 3) * 8;
    f32x4 d[2] = {{0.f, 0.f, 0.f, 0.f}, {0.f, 0.f, 0.f, 0.f}};
#pragma unroll
    for (int ks = 0; ks < 8; ++ks)
#pragma unroll
      for (int ct = 0; ct < 2; ++ct) {
        const v2u b0 = trread(sl + ks * 2048 + ct * 32), b1 = trread(sl + ks * 2048 + ct * 32 + 256);
        d[ct] = mfma(v4u{b0.x, b0.y, b1.x, b1.y}, xf[ks], d[ct]);
      }
    const f32x4 dv = r < 8 ? d[0] : d[1];
    const unsigned qo = (unsigned)W_QG + (unsigned)((((r & 7) * HKV + kap) * 2 + qk) * 512 + (qcol & 511) + (r >> 3) * 16 + 4 * g) * 8u;
    st16(rs, qo, v4u{f24(dv[0]) | TAG_LO, TAG_HI, f24(dv[1]) | TAG_LO, TAG_HI});
    st16(rs, qo + 16u, v4u{f24(dv[2]) | TAG_LO, TAG_HI, f24(dv[3]) | TAG_LO, TAG_HI});
    TS(1);
  } else if (c == 1) {
    lds_wait_ge(CT + C_TAGOK, 1u);
    T64 = ((u64)CT[C_TAG + 1] << 32) | CT[C_TAG];
    const v4u inv = {0u, ~(unsigned)T64, 0u, ~(unsigned)T64};
    const unsigned co = (unsigned)W_PG + (unsigned)(kap * 4 + (lane >> 5)) * PGB + (unsigned)(jg * 512 + (lane & 31) * 16);
    st16(rs, co, inv);
    st16(rs, co + 2u * PGB, inv);
    lds_wait_ge(CT + C_LANDED, 2u);
    LAS unsigned char* sl = L + L_RING + 7 * SLOT + g * 1024 + (r >> 2) * 64 + (r & 3) * 8;
    f32x4 d[4] = {{0.f, 0.f, 0.f, 0.f}, {0.f, 0.f, 0.f, 0.f}, {0.f, 0.f, 0.f, 0.f}, {0.f, 0.f, 0.f, 0.f}};
#pragma unroll
    for (int ks = 0; ks < 4; ++ks)
#pragma unroll
      for (int mc = 0; mc < 4; ++mc) {
        LAS unsigned char* q = sl + ks * 4096 + (mc >> 1) * 512 + (mc & 1) * 32;
        const v2u b0 = trread(q), b1 = trread(q + 256);
        d[mc] = mfma(v4u{b0.x, b0.y, b1.x, b1.y}, xf[ks], d[mc]);
      }
    LDS_WAIT();
    CT[C_GUFREE] = 1u;
    if (has_ffn) {
      const f32x4 dg = r < 8 ? d[0] : d[1], du = r < 8 ? d[2] : d[3];
      const unsigned go = (unsigned)W_GP + (unsigned)((fcg * 4 + 2 * (r >> 3) + (g >> 1)) * 2048 + fk * 512 + (r & 7) * 32 + (g & 1) * 16);
      st16(rs, go, __builtin_bit_cast(v4u, dg));
      st16(rs, go + 256u, __builtin_bit_cast(v4u, du));
    }
    VMWAIT(0);
    if (lane == 0) __hip_atomic_store(ws64 + W_FLAG / 8 + bid, T64, RLX_AGENT);
  } else {
    lds_wait_ge(CT + C_TAGOK, 1u);
    T64 = ((u64)CT[C_TAG + 1] << 32) | CT[C_TAG];
  }
  const unsigned T32 = (unsigned)T64;
  const v4u ones = {0x3F803F80u, 0x3F803F80u, 0x3F803F80u, 0x3F803F80u};
  LAS unsigned char* kbase = L + L_RING + c * SLOT + r * 256;
  LAS unsigned char* vbase = L + L_RING + (4 + c) * SLOT + (4 * g + (r >> 2)) * 256 + (r & 3) * 8;
  const int vx = 4 * (g & 1) + (r >> 2);

  v4u qf[4];
  f32x4 acc[8], accl = {0.f, 0.f, 0.f, 0.f};
#pragma unroll
  for (int m = 0; m < 8; ++m) acc[m] = f32x4{0.f, 0.f, 0.f, 0.f};
  float mrun = -INFINITY, fown = 0.f;
  f32x4 basea = {0.f, 0.f, 0.f, 0.f}, baseb = {0.f, 0.f, 0.f, 0.f};
#pragma unroll 1
  for (int lap = 0; lap < NTILE / 4; ++lap) {
    const unsigned n = 4u * lap + c;
    lds_wait_ge(CT + C_LANDED, 3u + 2u * n);
    v4u kf[4][4];
#pragma unroll
    for (int T = 0; T < 4; ++T)
#pragma unroll
      for (int ks = 0; ks < 4; ++ks) kf[T][ks] = *(const LAS v4u*)(kbase + T * 4096 + (((4 * ks + g) ^ r) * 16));
    LDS_WAIT();
    CT[C_DONE + c] = (unsigned)(2 * lap + 1);
#pragma unroll
    for (int T = 0; T < 4; ++T) asm volatile("" : "+v"(kf[T][0]), "+v"(kf[T][1]), "+v"(kf[T][2]), "+v"(kf[T][3]) :: "memory");
    v4u vf[2][8];
#define READ_V() do { \
      lds_wait_ge(CT + C_LANDED, 4u + 2u * n); \
      _Pragma("unroll") for (int ks = 0; ks < 2; ++ks) \
        _Pragma("unroll") for (int m = 0; m < 8; ++m) { \
          LAS unsigned char* p = vbase + ks * 8192 + ((m ^ vx) * 32); \
          const v2u t0 = trread(p), t1 = trread(p + 4096); \
          vf[ks][m] = v4u{t0.x, t0.y, t1.x, t1.y}; \
        } \
      LDS_WAIT(); \
      CT[C_DONE + c] = (unsigned)(2 * lap + 2); \
      _Pragma("unroll") for (int ks = 0; ks < 2; ++ks) { \
        asm volatile("" : "+v"(vf[ks][0]), "+v"(vf[ks][1]), "+v"(vf[ks][2]), "+v"(vf[ks][3]) :: "memory"); \
        asm volatile("" : "+v"(vf[ks][4]), "+v"(vf[ks][5]), "+v"(vf[ks][6]), "+v"(vf[ks][7]) :: "memory"); \
      } } while (0)
    if (lap == 0) {
      READ_V();
      lds_wait_ge(CT + C_QRDY, 1u);
#pragma unroll
      for (int ks = 0; ks < 4; ++ks) qf[ks] = *(const LAS v4u*)(L + L_Q + ((r & 3) * 128 + 32 * ks + 8 * g) * 2);
    }
    f32x4 sc[4];
#pragma unroll
    for (int T = 0; T < 4; ++T) {
      f32x4 d = {0.f, 0.f, 0.f, 0.f};
#pragma unroll
      for (int ks = 0; ks < 4; ++ks) d = mfma(kf[T][ks], qf[ks], d);
      sc[T] = d;
    }
    float tm = fmaxf(fmaxf(sc[0][0], sc[0][1]), fmaxf(sc[0][2], sc[0][3]));
#pragma unroll
    for (int T = 1; T < 4; ++T) tm = fmaxf(tm, fmaxf(fmaxf(sc[T][0], sc[T][1]), fmaxf(sc[T][2], sc[T][3])));
    tm = xrow_max(tm);
    if (__any(tm > mrun)) {
      const float mn = fmaxf(mrun, tm);
      const float al = __builtin_amdgcn_exp2f((mrun - mn) * SC2);
#pragma unroll
      for (int m = 0; m < 8; ++m) acc[m] *= al;
      accl *= al;
      mrun = mn;
    }
    const float mo = -mrun * SC2;
    v4u pf[2];
#pragma unroll
    for (int T = 0; T < 4; ++T) {
      float p[4];
#pragma unroll
      for (int e = 0; e < 4; ++e) p[e] = __builtin_amdgcn_exp2f(fmaf(sc[T][e], SC2, mo));
      pf[T >> 1][(T & 1) * 2] = cvtpk(p[0], p[1]);
      pf[T >> 1][(T & 1) * 2 + 1] = cvtpk(p[2], p[3]);
    }
    accl = mfma(ones, pf[0], accl);
    accl = mfma(ones, pf[1], accl);
    asm volatile("" : "+v"(pf[0]), "+v"(pf[1]), "+v"(accl) :: "memory");
    if (n == NTILE - 1) {
      float mw[3], lw[3]; f32x4 oa[3], ob[3];
#pragma unroll
      for (int w = 0; w < 3; ++w) {
        lds_wait_ge(CT + C_MRG + w, 1u);
        const LAS float* rw = (const LAS float*)(L + L_RING + w * SLOT);
        mw[w] = rw[512 + (r & 3)]; lw[w] = rw[516 + (r & 3)];
        oa[w] = *(const LAS f32x4*)(rw + (r & 3) * 128 + 32 * (r >> 2) + 4 * g); ob[w] = *(const LAS f32x4*)(rw + (r & 3) * 128 + 32 * (r >> 2) + 16 + 4 * g);
      }
      const unsigned m24 = f24(fmaxf(fmaxf(mrun, mw[0]), fmaxf(mw[1], mw[2])) * SC2);
      const float Mr = __uint_as_float(m24 << 8);
      fown = __builtin_amdgcn_exp2f(fmaf(mrun, SC2, -Mr));
      float Ls = fown * accl[0];
#pragma unroll
      for (int w = 0; w < 3; ++w) {
        const float f = __builtin_amdgcn_exp2f(fmaf(mw[w], SC2, -Mr));
        Ls = fmaf(f, lw[w], Ls); basea += f * oa[w]; baseb += f * ob[w];
      }
      const float inv = 1.f / Ls;
      fown *= inv; basea *= inv; baseb *= inv;
      if (lane < 4) st16(rs, (unsigned)W_PG + (unsigned)(kap * 4 + lane) * PGB + (unsigned)(16384 + jg * 16), v4u{m24 | TAG_LO, TAG_HI, f24(Ls) | TAG_LO, TAG_HI});
    }
    if (lap) READ_V();
#pragma unroll
    for (int ks = 0; ks < 2; ++ks)
#pragma unroll
      for (int m = 0; m < 8; ++m) acc[m] = mfma(vf[ks][m], pf[ks], acc[m]);
  }
  TS(4);
  if (c == 3) {
    const int u = r >> 2;
    const f32x4 sa = u == 0 ? acc[0] : u == 1 ? acc[2] : u == 2 ? acc[4] : acc[6], sb = u == 0 ? acc[1] : u == 1 ? acc[3] : u == 2 ? acc[5] : acc[7];
    const f32x4 xa = fown * sa + basea, xb = fown * sb + baseb;
    const unsigned po = (unsigned)W_PG + (unsigned)(kap * 4 + (r & 3)) * PGB + (unsigned)(jg * 512 + (16 * u + 2 * g) * 8);
    st16(rs, po, v4u{cvtpk(xa[0], xa[1]), T32, cvtpk(xa[2], xa[3]), T32});
    st16(rs, po + 64u, v4u{cvtpk(xb[0], xb[1]), T32, cvtpk(xb[2], xb[3]), T32});
    TS(2);
  } else {
    LAS float* rec = (LAS float*)(L + L_RING + c * SLOT);
    if (r < 4) {
#pragma unroll
      for (int m = 0; m < 8; ++m) *(LAS f32x4*)(rec + r * 128 + 16 * m + 4 * g) = acc[m];
      if (g == 0) { rec[512 + r] = mrun; rec[516 + r] = accl[0]; }
    }
    LDS_WAIT();
    CT[C_MRG + c] = 1u;
  }
  lds_wait_ge(CT + C_CLEAN, 1u);
  {
    LAS unsigned* stw = (LAS unsigned*)(L + L_RING + c * SLOT + 4096) + lane;
    gu64* pgp = (gu64*)(a.ws + W_PG + (size_t)ht * PGB + c * 4096);
    unsigned need = 0xffu, ca = 0u, cb = 0u;
    bool pre = false;
    v4u bw[4]; f32x4 dff = {0.f, 0.f, 0.f, 0.f};
    u64 va[8], vb[8];
#define H1_ISSUE(v, cm) do { cm = need & (CT[C_SEEN] >> (8 * c)); \
      if (cm) { _Pragma("unroll") for (int i = 0; i < 8; ++i) v[i] = __hip_atomic_load(((cm >> i) & 1u) ? pgp + i * 64 + lane : pgp, RLX_AGENT); } } while (0)
#define H1_CHECK(v, cm) do { if (cm) { _Pragma("unroll") for (int i = 0; i < 8; ++i) \
      if (((cm & need) >> i) & 1u) { if (__all((unsigned)(v[i] >> 32) == T32)) { stw[i * (STG_ROW / 4)] = (unsigned)v[i]; need &= ~(1u << i); } } } } while (0)
    _Pragma("unroll 1") for (unsigned sp = 0; sp < LSPIN; ++sp) {
      H1_ISSUE(va, ca);
      if (!pre && CT[C_WOK] != 0u) {
        CBAR();
        LAS unsigned char* wb = L + L_A + (8 * g + (r >> 2)) * 128 + c * 32 + (r & 3) * 8;
#pragma unroll
        for (int ks = 0; ks < 4; ++ks) { const v2u t0 = trread(wb + ks * 4096), t1 = trread(wb + ks * 4096 + 512); bw[ks] = v4u{t0.x, t0.y, t1.x, t1.y}; }
#pragma unroll
        for (int ks = 0; ks < 2; ++ks) {
          const v2u t0 = trread(wb + SLOT + ks * 4096), t1 = trread(wb + SLOT + ks * 4096 + 512);
          const v4u hf = *(const LAS v4u*)(L + L_HB + (4 * (r & 1) + (r >> 2)) * HB_ROW + 64 * ks + 16 * g);
          dff = mfma(hf, v4u{t0.x, t0.y, t1.x, t1.y}, dff);
        }
        pre = true;
      }
      H1_CHECK(vb, cb);
      if (need == 0u && pre) break;
      __builtin_amdgcn_s_sleep(H1_SLEEP);
      H1_ISSUE(vb, cb);
      H1_CHECK(va, ca);
      if (need == 0u && pre) break;
      __builtin_amdgcn_s_sleep(H1_SLEEP);
    }
    LDS_WAIT();
    CT[C_STG + c] = 1u;
#pragma unroll 1
    for (unsigned sp = 0; sp < LSPIN; ++sp) { const v4u fl = *(volatile LAS v4u*)(CT + C_STG); if (fl.x & fl.y & fl.z & fl.w) break; }
    CBAR();
    if (c == 3) TS(3);
    const LAS float* mls = (const LAS float*)(L + L_ML);
    LAS unsigned char* stg = L + L_RING + (r >> 3) * SLOT + 4096 + (r & 7) * STG_ROW + 16 * g;
#pragma unroll
    for (int rt = 0; rt < 2; ++rt) {
      f32x4 d = {0.f, 0.f, 0.f, 0.f};
#pragma unroll
      for (int ks = 0; ks < 4; ++ks) d = mfma(*(const LAS v4u*)(stg + 2 * rt * SLOT + 64 * ks), bw[ks], d);
      const f32x4 m01 = *(const LAS f32x4*)(mls + 32 * rt + 8 * g), m23 = *(const LAS f32x4*)(mls + 32 * rt + 8 * g + 4);
      const float Mx = fmaxf(fmaxf(m01[0], m01[2]), fmaxf(m23[0], m23[2]));
      const float w0 = __builtin_amdgcn_exp2f(m01[0] - Mx) * m01[1], w1 = __builtin_amdgcn_exp2f(m01[2] - Mx) * m01[3];
      const float w2 = __builtin_amdgcn_exp2f(m23[0] - Mx) * m23[1], w3 = __builtin_amdgcn_exp2f(m23[2] - Mx) * m23[3];
      const float gate = __uint_as_float(CT[C_G + 4 * rt + g]);
      const float att = ((w0 * d[0] + w1 * d[1]) + (w2 * d[2] + w3 * d[3])) / ((w0 + w1) + (w2 + w3));
      const float t = fmaf(gate, att, (1.f - gate) * dff[rt]);
      __hip_atomic_store(ws64 + W_OG / 8 + ((size_t)((4 * rt + g) * 32 + 4 * n8 + c) * 32 + ht) * 16 + r, ((u64)TAG_HI << 32) | (f24(t) | TAG_LO), RLX_AGENT);
    }
  }
  if (c == 0) TS(2);
  if (c == 0) {
    const unsigned oo = (unsigned)W_OG + (unsigned)(bid * 4096 + lane * 16);
    float t0 = 0.f, t1 = 0.f;
    v4u va[4], vb[4];
    bool ok;
#define H2_ISSUE(v) do { _Pragma("unroll") for (int i = 0; i < 4; ++i) v[i] = ld16(rs, oo + i * 1024u); } while (0)
#define H2_CHECK(v) do { t0 = 0.f; t1 = 0.f; ok = true; \
      _Pragma("unroll") for (int i = 0; i < 4; ++i) { ok &= TAG_OK(v[i].x, v[i].y) && TAG_OK(v[i].z, v[i].w); t0 += __uint_as_float(v[i].x << 8); t1 += __uint_as_float(v[i].z << 8); } } while (0)
    H2_ISSUE(va);
    _Pragma("unroll 1") for (unsigned sp = 0; sp < GSPIN; ++sp) {
      __builtin_amdgcn_s_sleep(H2_SLEEP);
      H2_ISSUE(vb);
      H2_CHECK(va);
      if (__all(ok)) break;
      __builtin_amdgcn_s_sleep(H2_SLEEP);
      H2_ISSUE(va);
      H2_CHECK(vb);
      if (__all(ok)) break;
    }
    t0 += dpp<0x128>(t0); t1 += dpp<0x128>(t1);
    t0 = xrow_sum(t0); t1 = xrow_sum(t1);
    if (lane < 8) ((GAS unsigned*)a.out)[bid * 8 + lane] = cvtpk(t0, t1);
    TS(3);
    if (bid == 0 && lane == 0) __hip_atomic_store(ws64 + W_EPOCH / 8, T64, RLX_AGENT);
  }
  TS_FLUSH(wave);
}
}

#include <cstdio>
__global__ void k_report(const unsigned char* ws) {
  const unsigned long long* ts = (const unsigned long long*)(ws + W_END);
  if (threadIdx.x != 0) return;
  unsigned long long t0 = ~0ull;
  for (int b = 0; b < 256; ++b) { const unsigned long long v = ts[b * 72 + 5]; if (v < t0) t0 = v; }
  const int Lw[12] = {1, 0, 0, 5, 0, 0, 0, 4, 4, 4, 1, 1}, Li[12] = {1, 9, 10, 2, 1, 11, 2, 8, 2, 3, 2, 3};
  for (int l = 0; l < 12; ++l) {
    double mn = 1e30, mx = -1e30, sm = 0;
    for (int b = 0; b < 256; ++b) { const unsigned long long* t = &ts[(b * 6 + Lw[l]) * 12]; const double k = (double)(t[7] - t[5]) / (double)(t[6] - t[4]);
      const double x = ((double)(t[5] - t0) + (double)(long long)(t[Li[l]] - t[4]) * k) * 0.01; mn = x < mn ? x : mn; mx = x > mx ? x : mx; sm += x; }
    printf("STAMP %d: min %.1f avg %.1f max %.1f\n", l, mn, sm / 256, mx);
  }
}
extern "C" const size_t diag_ts_offset = W_END;
extern "C" void kernel_launch(void* const* d_in, const int* in_sizes, int n_in, void* d_out, int out_size, void* d_ws,
                              size_t ws_size, hipStream_t stream) {
  (void)in_sizes; (void)n_in; (void)out_size;
  if (ws_size < W_END) return;
  Args a;
  a.x = (const unsigned char*)d_in[0]; a.k = (const unsigned char*)d_in[1]; a.v = (const unsigned char*)d_in[2];
  a.wq = (const unsigned char*)d_in[3]; a.wo = (const unsigned char*)d_in[4];
  a.wg = (const unsigned char*)d_in[6]; a.wu = (const unsigned char*)d_in[7]; a.wd = (const unsigned char*)d_in[8];
  a.gw = (const unsigned char*)d_in[9];
  a.out = (unsigned char*)d_out; a.ws = (unsigned char*)d_ws;
  (void)hipFuncSetAttribute((const void*)fused_kernel, hipFuncAttributeMaxDynamicSharedMemorySize, L_BYTES);
  hipLaunchKernelGGL(fused_kernel, dim3(NWG), dim3(NTHR), L_BYTES, stream, a);
  hipLaunchKernelGGL(k_report, dim3(1), dim3(64), 0, stream, (const unsigned char*)d_ws);
  printf("PTRS x %p k %p v %p wq %p wo %p ws %p out %p\n", d_in[0], d_in[1], d_in[2], d_in[3], d_in[4], d_ws, d_out);
}
```

```cpp
#include <hip/hip_runtime.h>
#include <hip/hip_bf16.h>
#include <stdint.h>

#define GAS __attribute__((address_space(1)))
#define LAS __attribute__((address_space(3)))
typedef unsigned v4u __attribute__((ext_vector_type(4)));
typedef unsigned v2u __attribute__((ext_vector_type(2)));
typedef float f32x4 __attribute__((ext_vector_type(4)));
typedef float f32x2 __attribute__((ext_vector_type(2)));
typedef __bf16 bf16x8 __attribute__((ext_vector_type(8)));
typedef short s16x4 __attribute__((ext_vector_type(4)));
typedef unsigned long long u64;
typedef GAS u64 gu64;
#define RLX_AGENT __ATOMIC_RELAXED, __HIP_MEMORY_SCOPE_AGENT

namespace {
constexpr int NB = 8, DM = 512, HQ = 32, HKV = 8, SK = 8192, DH = 128, FF = 1792;
constexpr int NWG = 256, NTHR = 6 * 64;
constexpr int NTILE = 32;
#define SW 16
#define WO_EARLY 8
#define QG_AT 6u
#define H1_SLEEP 6
#define H2_SLEEP 6
#define ML_SLEEP 8
constexpr int SLOT = 16384;
constexpr int L_RING = 0, L_A = 8 * SLOT, L_B = L_A + SLOT, L_Q = L_B + 8192, L_HB = L_Q + 1024, L_ML = L_HB + 2048, L_CTRL = L_ML + 256, L_BYTES = L_CTRL + 256;
constexpr int STG_ROW = 272;
constexpr int HB_ROW = 144;
constexpr int C_LANDED = 0, C_GUFREE = 1, C_QRDY = 2, C_SEEN = 3, C_DONE = 4, C_MRG = 8, C_STG = 12, C_TAG = 16, C_TAGOK = 18, C_WOK = 19, C_CLEAN = 20, C_G = 24;
static_assert(L_BYTES <= 160 * 1024, "LDS");
constexpr size_t W_EPOCH = 0, W_G = 256, W_FLAG = 4096;
constexpr size_t W_QG = 8192;
constexpr size_t W_GP = W_QG + (size_t)2 * NB * 4096 * 8;
constexpr size_t W_HG = W_GP + (size_t)224 * 2048;
constexpr size_t W_PG = W_HG + (size_t)225 * 512 + 512;
constexpr unsigned PGB = 16384 + 512;
constexpr size_t W_OG = W_PG + (size_t)HQ * PGB;
constexpr size_t W_END = W_OG + (size_t)NWG * 4096;
static_assert(W_END < (1u << 30) && W_PG % 1024 == 0, "buffer offsets are int");
constexpr u64 EPOCH_STEP = 0x9E3779B97F4A7C15ull;
constexpr unsigned GSPIN = 1u << 15, LSPIN = 1u << 18;
constexpr float SC2 = 0.08838834764831845f * 1.4426950408889634f;

struct Args {
  const unsigned char *x, *k, *v, *wq, *wo, *wg, *wu, *wd, *gw;
  unsigned char *out, *ws;
};

__device__ __forceinline__ f32x4 mfma(v4u a, v4u b, f32x4 c) {
  return __builtin_amdgcn_mfma_f32_16x16x32_bf16(__builtin_bit_cast(bf16x8, a), __builtin_bit_cast(bf16x8, b), c, 0, 0, 0);
}
__device__ __forceinline__ v2u trread(LAS unsigned char* p) {
  return __builtin_bit_cast(v2u, __builtin_amdgcn_ds_read_tr16_b64_v4i16((LAS s16x4*)p));
}
__device__ __forceinline__ unsigned cvtpk(float lo, float hi) {
  typedef __bf16 bf16x2 __attribute__((ext_vector_type(2)));
  bf16x2 r; r[0] = (__bf16)lo; r[1] = (__bf16)hi; return __builtin_bit_cast(unsigned, r);
}
template <int CTRL> __device__ __forceinline__ float dpp(float x) {
  return __builtin_bit_cast(float, __builtin_amdgcn_mov_dpp(__builtin_bit_cast(int, x), CTRL, 0xf, 0xf, true));
}
__device__ __forceinline__ float xrow_max(float x) {
  auto s = __builtin_amdgcn_permlane16_swap(__float_as_uint(x), __float_as_uint(x), false, false);
  x = fmaxf(__uint_as_float(s[0]), __uint_as_float(s[1]));
  auto t = __builtin_amdgcn_permlane32_swap(__float_as_uint(x), __float_as_uint(x), false, false);
  return fmaxf(__uint_as_float(t[0]), __uint_as_float(t[1]));
}
__device__ __forceinline__ float xrow_sum(float x) {
  auto s = __builtin_amdgcn_permlane16_swap(__float_as_uint(x), __float_as_uint(x), false, false);
  x = __uint_as_float(s[0]) + __uint_as_float(s[1]);
  auto t = __builtin_amdgcn_permlane32_swap(__float_as_uint(x), __float_as_uint(x), false, false);
  return __uint_as_float(t[0]) + __uint_as_float(t[1]);
}
__device__ __forceinline__ float bflo(unsigned u) { return __uint_as_float(u << 16); }
__device__ __forceinline__ float bfhi(unsigned u) { return __uint_as_float(u & 0xffff0000u); }

__device__ __forceinline__ v4u ld16(__amdgpu_buffer_rsrc_t rs, unsigned off) { return __builtin_amdgcn_raw_buffer_load_b128(rs, (int)off, 0, 16); }
__device__ __forceinline__ void st16(__amdgpu_buffer_rsrc_t rs, unsigned off, v4u v) { __builtin_amdgcn_raw_buffer_store_b128(v, rs, (int)off, 0, 16); }
__device__ __forceinline__ unsigned f24(float v) { return (__float_as_uint(v) + 0x80u) >> 8; }
#define TAG_LO ((unsigned)T64 << 24)
#define TAG_HI ((unsigned)(T64 >> 8))
#define TAG_OK(lo, hi) ((hi) == TAG_HI && ((lo) ^ TAG_LO) < 0x1000000u)
#define CBAR() asm volatile("" ::: "memory")
#define LDS_WAIT() asm volatile("s_waitcnt lgkmcnt(0)" ::: "memory")
#define KPOL " nt"
#define VPOL " nt"
#define WPOL " nt"
#define DMA4(sbase, dst, v0, v1, v2, v3, POL)                                                                    \
  asm volatile("s_mov_b32 m0, %5\n\ts_nop 0\n\t"                                                                 \
               "global_load_lds_dwordx4 %0, %4" POL "\n\t"                                                       \
               "global_load_lds_dwordx4 %1, %4 offset:1024" POL "\n\t"                                           \
               "global_load_lds_dwordx4 %2, %4 offset:2048" POL "\n\t"                                           \
               "global_load_lds_dwordx4 %3, %4 offset:3072" POL                                                  \
               :: "v"(v0), "v"(v1), "v"(v2), "v"(v3), "s"(sbase), "s"(dst) : "memory")
#define DMA1S(sbase, voff, dst)                                                                                  \
  asm volatile("s_mov_b32 m0, %2\n\ts_nop 0\n\tglobal_load_lds_dwordx4 %0, %1" WPOL :: "v"(voff), "s"(sbase), "s"(dst) : "memory")
#define DMA1V(vaddr, dst)                                                                                        \
  asm volatile("s_mov_b32 m0, %1\n\ts_nop 0\n\tglobal_load_lds_dwordx4 %0, off" WPOL :: "v"(vaddr), "s"(dst) : "memory")
#define VMWAIT(n) asm volatile("s_waitcnt vmcnt(" #n ")" ::: "memory")
#define VMWAITN(n) asm volatile("s_waitcnt vmcnt(%0)" :: "i"(n) : "memory")
#define TS(i) do { } while (0)
#define TS_FLUSH(w) do { } while (0)

__device__ __forceinline__ void lds_wait_ge(volatile LAS unsigned* w, unsigned need) {
#pragma unroll 1
  for (unsigned sp = 0; *w < need && sp < LSPIN; ++sp) __builtin_amdgcn_s_sleep(1);
  CBAR();
}

__global__ void __launch_bounds__(NTHR, 2) fused_kernel(Args a) {
  extern __shared__ __attribute__((aligned(16))) unsigned char smem[];
  LAS unsigned char* L = (LAS unsigned char*)smem;
  volatile LAS unsigned* CT = (volatile LAS unsigned*)(L + L_CTRL);
  const int tid = threadIdx.x, lane = tid & 63, wave = __builtin_amdgcn_readfirstlane(tid >> 6);
  const int bid = blockIdx.x;
  const int kap = bid >> 5, jg = bid & 31, b = jg >> 2, s = jg & 3;
  const int qcol = kap * 512 + (((jg & 7) << 1) | ((jg >> 3) & 1)) * 32, qk = jg >> 4;
  const bool has_ffn = (bid >> 3) < 28;
  const int fi = has_ffn ? (bid >> 3) : 27;
  const int fcg = (bid & 7) * 7 + (fi >> 2), fk = fi & 3;
  const int hh = jg >> 3, ht = kap * 4 + hh, n8 = jg & 7;
  const int g = lane >> 4, r = lane & 15;
  gu64* ws64 = (gu64*)a.ws;
  const __amdgpu_buffer_rsrc_t rs = __builtin_amdgcn_make_buffer_rsrc((void*)a.ws, 0, (int)W_END, 0x00020000);
  if (wave == 0) {
    __builtin_amdgcn_s_waitcnt(0);
    __builtin_amdgcn_s_barrier();
    CBAR();
    __builtin_amdgcn_s_setprio(3);
    const unsigned lb = (unsigned)(size_t)L;
    {
      const unsigned voff = (unsigned)(lane >> 2) * 8192u + (unsigned)(lane & 3) * 16u;
      const unsigned char* sb = a.wq + (size_t)qk * 256 * 8192 + (size_t)qcol * 2;
#pragma unroll 1
      for (int j = 0; j < 16; ++j) DMA1S(sb + (size_t)j * 16 * 8192, voff, lb + L_A + j * 1024);
    }
    {
      const unsigned char* p = ((lane & 32) ? a.wu : a.wg) + (size_t)(fk * 128 + ((lane >> 2) & 7)) * (FF * 2) + (size_t)fcg * 64 + (lane & 3) * 16;
#pragma unroll 1
      for (int j = 0; j < 16; ++j) DMA1V(p + (size_t)j * 8 * (FF * 2), lb + L_RING + 7 * SLOT + j * 1024);
    }
    unsigned ko[4], vo[2];
#pragma unroll
    for (int j = 0; j < 4; ++j) ko[j] = (unsigned)g * 256u + (unsigned)((r ^ g ^ (4 * j)) * 16);
#pragma unroll
    for (int j = 0; j < 2; ++j) vo[j] = (unsigned)g * 256u + (unsigned)((r ^ (2 * (4 * j + g))) * 16);
    const size_t seq_off = (size_t)(b * HKV + kap) * SK * 256;
    const unsigned char* kb = a.k + seq_off;
    const unsigned char* vb = a.v + seq_off;
    unsigned seen = 0;
#pragma unroll 1
    for (int n = 0; n < NTILE; ++n) {
      const int tile = ((n + 4 * b) & 31) * 4 + s;
      const int cs = n & 3;
#pragma unroll
      for (int hv = 0; hv < 2; ++hv) {
        const unsigned f = 2u + 2u * n + hv;
        if (n >= 4 || (n == 3 && hv == 1)) {
          volatile LAS unsigned* fw = n >= 4 ? (CT + C_DONE + cs) : (CT + C_GUFREE);
          const unsigned need = n >= 4 ? (unsigned)(2 * (n >> 2) - 1 + hv) : 1u;
          if (n < 4 || seen < need) {
            if (*fw < need) {
              VMWAIT(0); CT[C_LANDED] = f;
              lds_wait_ge(fw, need);
            }
          }
        }
        const unsigned char* sb = (hv ? vb : kb) + (size_t)tile * SLOT;
        const unsigned dst = lb + L_RING + (hv * 4 + cs) * SLOT;
        if (hv == 0) { DMA4(sb, dst, ko[0], ko[1], ko[2], ko[3], KPOL); DMA4(sb + 4096, dst + 4096, ko[0], ko[1], ko[2], ko[3], KPOL); }
        else         { DMA4(sb, dst, vo[0], vo[1], vo[0], vo[1], VPOL); DMA4(sb + 4096, dst + 4096, vo[0], vo[1], vo[0], vo[1], VPOL); }
        if (n) VMWAITN(SW);
        if (hv == 0) { DMA4(sb + 8192, dst + 8192, ko[0], ko[1], ko[2], ko[3], KPOL); DMA4(sb + 12288, dst + 12288, ko[0], ko[1], ko[2], ko[3], KPOL); }
        else         { DMA4(sb + 8192, dst + 8192, vo[0], vo[1], vo[0], vo[1], VPOL); DMA4(sb + 12288, dst + 12288, vo[0], vo[1], vo[0], vo[1], VPOL); }
        const unsigned nx = CT[C_DONE + (hv ? ((cs + 1) & 3) : cs)];
        if (n == 0) { VMWAIT(32); CT[C_LANDED] = f - 1; }
        else        { VMWAITN(SW); CT[C_LANDED] = SW <= 16 ? f : SW <= 32 ? f - 1 : f - 2; }
        seen = __builtin_amdgcn_readfirstlane(nx);
        if (f == 12) TS(1);
        if (f == 3) TS(5);
        if (f == 6) TS(6);
        if (f == 34) TS(7);
      }
    }
    {
      const unsigned voff = (unsigned)(lane >> 3) * 1024u + (unsigned)(lane & 7) * 16u;
      const unsigned char* sb = a.wo + (size_t)ht * 128 * 1024 + (size_t)n8 * 128;
      const unsigned char* sd = a.wd + (size_t)ht * 56 * 1024 + (size_t)n8 * 128;
      if (SW > 32) { VMWAIT(32); CT[C_LANDED] = 2u * NTILE; }
      if (SW > 16) { VMWAIT(16); CT[C_LANDED] = 1u + 2u * NTILE; }
#pragma unroll 1
      for (int j = 0; j < 23; ++j) {
        DMA1S(j < 16 ? sb + (size_t)j * 8192 : sd + (size_t)(j - 16) * 8192, voff, lb + L_A + j * 1024);
        if (j == WO_EARLY - 1) { VMWAITN(WO_EARLY); CT[C_LANDED] = 2u + 2u * NTILE; }
      }
      TS(2);
      VMWAIT(0); CT[C_WOK] = 1u;
      TS(3);
      TS_FLUSH(0);
    }
    return;
  }

  const int c = wave - 1;
  u64 T64 = 0;
  v4u xf[8];
  if (wave == 5) T64 = __hip_atomic_load(ws64 + W_EPOCH / 8, RLX_AGENT);
  if (wave <= 2) {
    const GAS v4u* xp = (const GAS v4u*)(a.x + (size_t)(r & 7) * 1024 + (wave == 1 ? qk * 512 : fk * 256) + g * 16);
#pragma unroll
    for (int ks = 0; ks < 8; ++ks) xf[ks] = xp[(wave == 1 ? ks : (ks & 3)) * 4];
  }
  if (wave == 3) ((LAS unsigned*)(L + L_CTRL))[lane] = 0u;
  if (wave == 4) ((LAS v4u*)(L + L_B + 7168))[lane] = v4u{0u, 0u, 0u, 0u};
  LDS_WAIT();
  __builtin_amdgcn_s_barrier();
  CBAR();

  if (wave == 5) {
    T64 += EPOCH_STEP;
    if (lane == 0) { CT[C_TAG] = (unsigned)T64; CT[C_TAG + 1] = (unsigned)(T64 >> 32); LDS_WAIT(); CT[C_TAGOK] = 1u; }
    if (bid == NWG - 1) {
      const int gb = lane >> 3, part = lane & 7;
      const GAS v4u* xp = (const GAS v4u*)(a.x + (size_t)gb * 1024 + part * 128);
      const GAS v4u* gp = (const GAS v4u*)(a.gw + part * 128);
      float ga = 0.f;
#pragma unroll
      for (int i = 0; i < 8; ++i) {
        const v4u xv = xp[i], gv = gp[i];
#pragma unroll
        for (int e = 0; e < 4; ++e) { ga = fmaf(bflo(xv[e]), bflo(gv[e]), ga); ga = fmaf(bfhi(xv[e]), bfhi(gv[e]), ga); }
      }
      ga += dpp<0xB1>(ga); ga += dpp<0x4E>(ga); ga += dpp<0x141>(ga);
      const float gate = 1.f / (1.f + __expf(-ga));
      if (part == 0) __hip_atomic_store(ws64 + W_G / 8 + gb, ((u64)TAG_HI << 32) | (f24(gate) | TAG_LO), RLX_AGENT);
    }
    {
      lds_wait_ge(CT + C_LANDED, QG_AT);
      const unsigned qo = (unsigned)W_QG + (unsigned)((b * HKV + kap) * 8192 + lane * 16);
      _Pragma("unroll 1") for (unsigned sp = 0; sp < GSPIN; ++sp) {
        v4u v[8]; bool ok = true;
#pragma unroll
        for (int i = 0; i < 8; ++i) v[i] = ld16(rs, qo + i * 1024u);
#pragma unroll
        for (int i = 0; i < 4; ++i) {
          ok &= TAG_OK(v[i].x, v[i].y) && TAG_OK(v[i].z, v[i].w) && TAG_OK(v[i + 4].x, v[i + 4].y) && TAG_OK(v[i + 4].z, v[i + 4].w);
          ((LAS unsigned*)(L + L_Q))[i * 64 + lane] = cvtpk(__uint_as_float(v[i].x << 8) + __uint_as_float(v[i + 4].x << 8), __uint_as_float(v[i].z << 8) + __uint_as_float(v[i + 4].z << 8));
        }
        if (__all(ok)) break;
        __builtin_amdgcn_s_sleep(8);
      }
      LDS_WAIT(); CT[C_QRDY] = 1u;
      TS(2);
    }
    {
      lds_wait_ge(CT + C_LANDED, 12u);
      const int fb = lane < 32 ? kap * 32 + lane : (bid & 7) + 8 * ((fi & ~3) + (lane & 3));
      _Pragma("unroll 1") for (unsigned sp = 0; sp < GSPIN; ++sp) {
        if (__all(__hip_atomic_load(ws64 + W_FLAG / 8 + fb, RLX_AGENT) == T64)) break;
        __builtin_amdgcn_s_sleep(32);
      }
      CBAR();
    }
    if (has_ffn) {
      const unsigned go = (unsigned)W_GP + (unsigned)((fcg * 4 + fk) * 2048 + lane * 16);
      const f32x4 p0 = __builtin_bit_cast(f32x4, ld16(rs, go)), p1 = __builtin_bit_cast(f32x4, ld16(rs, go + 1024u));
      unsigned hw[4];
#pragma unroll
      for (int e = 0; e < 4; ++e) {
        const unsigned t = __float_as_uint(p0[e] + p1[e]);
        auto s32 = __builtin_amdgcn_permlane32_swap(t, t, false, false);
        const unsigned tot = __float_as_uint(__uint_as_float(s32[0]) + __uint_as_float(s32[1]));
        auto s16 = __builtin_amdgcn_permlane16_swap(tot, tot, false, false);
        const float gt = __uint_as_float(s16[0]), up = __uint_as_float(s16[1]);
        const float u = 0.7978845608028654f * (gt + 0.044715f * gt * gt * gt);
        const float th = 1.f - 2.f / (__expf(2.f * u) + 1.f);
        hw[e] = f24(0.5f * gt * (1.f + th) * up) | TAG_LO;
      }
      if (lane < 16) {
        const unsigned ho = (unsigned)W_HG + (unsigned)((fcg * 4 + fk) * 512 + lane * 32);
        st16(rs, ho, v4u{hw[0], TAG_HI, hw[1], TAG_HI});
        st16(rs, ho + 16u, v4u{hw[2], TAG_HI, hw[3], TAG_HI});
      }
    }
    {
      lds_wait_ge(CT + C_LANDED, 20u);
      const unsigned ho = (unsigned)W_HG + (unsigned)(ht * 7 * 512 + lane * 16);
      _Pragma("unroll 1") for (unsigned sp = 0; sp < GSPIN; ++sp) {
        v4u v[4];
#pragma unroll
        for (int i = 0; i < 4; ++i) v[i] = ld16(rs, ho + i * 1024u);
        const u64 gv = __hip_atomic_load(ws64 + W_G / 8 + (lane & 7), RLX_AGENT);
        bool ok = TAG_OK((unsigned)gv, (unsigned)(gv >> 32));
#pragma unroll
        for (int i = 0; i < 4; ++i) {
          const bool pad = i == 3 && lane >= 32;
          ok &= pad || (TAG_OK(v[i].x, v[i].y) && TAG_OK(v[i].z, v[i].w));
          *(LAS unsigned*)(L + L_HB + ((lane >> 2) & 7) * HB_ROW + (2 * i + (lane >> 5)) * 16 + (lane & 3) * 4) =
              pad ? 0u : cvtpk(__uint_as_float(v[i].x << 8), __uint_as_float(v[i].z << 8));
        }
        if (lane < 8) CT[C_G + lane] = (unsigned)gv << 8;
        if (__all(ok)) break;
        __builtin_amdgcn_s_sleep(32);
      }
      LDS_WAIT();
      CT[C_CLEAN] = 1u;
      TS(1);
    }
    {
      lds_wait_ge(CT + C_LANDED, 2u * NTILE - 2u);
      gu64* mlp = (gu64*)(a.ws + W_PG + (size_t)ht * PGB + 16384) + lane;
      u64 ma = __hip_atomic_load(mlp, RLX_AGENT), mb;
#define ML_CHECK(v) do { const bool mk = TAG_OK((unsigned)v, (unsigned)(v >> 32)); if (mk) ((LAS unsigned*)(L + L_ML))[lane] = (unsigned)v << 8; \
        u64 k = __ballot(mk); k &= k >> 1; k &= 0x5555555555555555ull; \
        k = (k | (k >> 1)) & 0x3333333333333333ull; k = (k | (k >> 2)) & 0x0f0f0f0f0f0f0f0full; k = (k | (k >> 4)) & 0x00ff00ff00ff00ffull; \
        k = (k | (k >> 8)) & 0x0000ffff0000ffffull; k = (k | (k >> 16)) & 0xffffffffull; \
        sn = (unsigned)k; LDS_WAIT(); CT[C_SEEN] = sn; } while (0)
      unsigned sn;
      _Pragma("unroll 1") for (unsigned sp = 0; sp < GSPIN; ++sp) {
        __builtin_amdgcn_s_sleep(ML_SLEEP);
        mb = __hip_atomic_load(mlp, RLX_AGENT);
        ML_CHECK(ma);
        if (sn == ~0u) break;
        __builtin_amdgcn_s_sleep(ML_SLEEP);
        ma = __hip_atomic_load(mlp, RLX_AGENT);
        ML_CHECK(mb);
        if (sn == ~0u) break;
      }
    }
    TS_FLUSH(5);
    return;
  }

  if (c == 0) {
    lds_wait_ge(CT + C_TAGOK, 1u);
    T64 = ((u64)CT[C_TAG + 1] << 32) | CT[C_TAG];
    lds_wait_ge(CT + C_LANDED, 1u);
    TS(0);
    LAS unsigned char* sl = L + L_A + (8 * g + (r >> 2)) * 64 + (r & 3) * 8;
    f32x4 d[2] = {{0.f, 0.f, 0.f, 0.f}, {0.f, 0.f, 0.f, 0.f}};
#pragma unroll
    for (int ks = 0; ks < 8; ++ks)
#pragma unroll
      for (int ct = 0; ct < 2; ++ct) {
        const v2u b0 = trread(sl + ks * 2048 + ct * 32), b1 = trread(sl + ks * 2048 + ct * 32 + 256);
        d[ct] = mfma(v4u{b0.x, b0.y, b1.x, b1.y}, xf[ks], d[ct]);
      }
    const f32x4 dv = r < 8 ? d[0] : d[1];
    const unsigned qo = (unsigned)W_QG + (unsigned)((((r & 7) * HKV + kap) * 2 + qk) * 512 + (qcol & 511) + (r >> 3) * 16 + 4 * g) * 8u;
    st16(rs, qo, v4u{f24(dv[0]) | TAG_LO, TAG_HI, f24(dv[1]) | TAG_LO, TAG_HI});
    st16(rs, qo + 16u, v4u{f24(dv[2]) | TAG_LO, TAG_HI, f24(dv[3]) | TAG_LO, TAG_HI});
    TS(1);
  } else if (c == 1) {
    lds_wait_ge(CT + C_TAGOK, 1u);
    T64 = ((u64)CT[C_TAG + 1] << 32) | CT[C_TAG];
    const v4u inv = {0u, ~(unsigned)T64, 0u, ~(unsigned)T64};
    const unsigned co = (unsigned)W_PG + (unsigned)(kap * 4 + (lane >> 5)) * PGB + (unsigned)(jg * 512 + (lane & 31) * 16);
    st16(rs, co, inv);
    st16(rs, co + 2u * PGB, inv);
    lds_wait_ge(CT + C_LANDED, 2u);
    LAS unsigned char* sl = L + L_RING + 7 * SLOT + g * 1024 + (r >> 2) * 64 + (r & 3) * 8;
    f32x4 d[4] = {{0.f, 0.f, 0.f, 0.f}, {0.f, 0.f, 0.f, 0.f}, {0.f, 0.f, 0.f, 0.f}, {0.f, 0.f, 0.f, 0.f}};
#pragma unroll
    for (int ks = 0; ks < 4; ++ks)
#pragma unroll
      for (int mc = 0; mc < 4; ++mc) {
        LAS unsigned char* q = sl + ks * 4096 + (mc >> 1) * 512 + (mc & 1) * 32;
        const v2u b0 = trread(q), b1 = trread(q + 256);
        d[mc] = mfma(v4u{b0.x, b0.y, b1.x, b1.y}, xf[ks], d[mc]);
      }
    LDS_WAIT();
    CT[C_GUFREE] = 1u;
    if (has_ffn) {
      const f32x4 dg = r < 8 ? d[0] : d[1], du = r < 8 ? d[2] : d[3];
      const unsigned go = (unsigned)W_GP + (unsigned)((fcg * 4 + 2 * (r >> 3) + (g >> 1)) * 2048 + fk * 512 + (r & 7) * 32 + (g & 1) * 16);
      st16(rs, go, __builtin_bit_cast(v4u, dg));
      st16(rs, go + 256u, __builtin_bit_cast(v4u, du));
    }
    VMWAIT(0);
    if (lane == 0) __hip_atomic_store(ws64 + W_FLAG / 8 + bid, T64, RLX_AGENT);
  } else {
    lds_wait_ge(CT + C_TAGOK, 1u);
    T64 = ((u64)CT[C_TAG + 1] << 32) | CT[C_TAG];
  }
  const unsigned T32 = (unsigned)T64;
  const v4u ones = {0x3F803F80u, 0x3F803F80u, 0x3F803F80u, 0x3F803F80u};
  LAS unsigned char* kbase = L + L_RING + c * SLOT + r * 256;
  LAS unsigned char* vbase = L + L_RING + (4 + c) * SLOT + (4 * g + (r >> 2)) * 256 + (r & 3) * 8;
  const int vx = 4 * (g & 1) + (r >> 2);

  v4u qf[4];
  f32x4 acc[8], accl = {0.f, 0.f, 0.f, 0.f};
#pragma unroll
  for (int m = 0; m < 8; ++m) acc[m] = f32x4{0.f, 0.f, 0.f, 0.f};
  float mrun = -INFINITY, fown = 0.f;
  f32x4 basea = {0.f, 0.f, 0.f, 0.f}, baseb = {0.f, 0.f, 0.f, 0.f};
#pragma unroll 1
  for (int lap = 0; lap < NTILE / 4; ++lap) {
    const unsigned n = 4u * lap + c;
    lds_wait_ge(CT + C_LANDED, 3u + 2u * n);
    v4u kf[4][4];
#pragma unroll
    for (int T = 0; T < 4; ++T)
#pragma unroll
      for (int ks = 0; ks < 4; ++ks) kf[T][ks] = *(const LAS v4u*)(kbase + T * 4096 + (((4 * ks + g) ^ r) * 16));
    LDS_WAIT();
    CT[C_DONE + c] = (unsigned)(2 * lap + 1);
#pragma unroll
    for (int T = 0; T < 4; ++T) asm volatile("" : "+v"(kf[T][0]), "+v"(kf[T][1]), "+v"(kf[T][2]), "+v"(kf[T][3]) :: "memory");
    v4u vf[2][8];
#define READ_V() do { \
      lds_wait_ge(CT + C_LANDED, 4u + 2u * n); \
      _Pragma("unroll") for (int ks = 0; ks < 2; ++ks) \
        _Pragma("unroll") for (int m = 0; m < 8; ++m) { \
          LAS unsigned char* p = vbase + ks * 8192 + ((m ^ vx) * 32); \
          const v2u t0 = trread(p), t1 = trread(p + 4096); \
          vf[ks][m] = v4u{t0.x, t0.y, t1.x, t1.y}; \
        } \
      LDS_WAIT(); \
      CT[C_DONE + c] = (unsigned)(2 * lap + 2); \
      _Pragma("unroll") for (int ks = 0; ks < 2; ++ks) { \
        asm volatile("" : "+v"(vf[ks][0]), "+v"(vf[ks][1]), "+v"(vf[ks][2]), "+v"(vf[ks][3]) :: "memory"); \
        asm volatile("" : "+v"(vf[ks][4]), "+v"(vf[ks][5]), "+v"(vf[ks][6]), "+v"(vf[ks][7]) :: "memory"); \
      } } while (0)
    if (lap == 0) {
      READ_V();
      lds_wait_ge(CT + C_QRDY, 1u);
#pragma unroll
      for (int ks = 0; ks < 4; ++ks) qf[ks] = *(const LAS v4u*)(L + L_Q + ((r & 3) * 128 + 32 * ks + 8 * g) * 2);
    }
    f32x4 sc[4];
#pragma unroll
    for (int T = 0; T < 4; ++T) {
      f32x4 d = {0.f, 0.f, 0.f, 0.f};
#pragma unroll
      for (int ks = 0; ks < 4; ++ks) d = mfma(kf[T][ks], qf[ks], d);
      sc[T] = d;
    }
    float tm = fmaxf(fmaxf(sc[0][0], sc[0][1]), fmaxf(sc[0][2], sc[0][3]));
#pragma unroll
    for (int T = 1; T < 4; ++T) tm = fmaxf(tm, fmaxf(fmaxf(sc[T][0], sc[T][1]), fmaxf(sc[T][2], sc[T][3])));
    tm = xrow_max(tm);
    if (__any(tm > mrun)) {
      const float mn = fmaxf(mrun, tm);
      const float al = __builtin_amdgcn_exp2f((mrun - mn) * SC2);
#pragma unroll
      for (int m = 0; m < 8; ++m) acc[m] *= al;
      accl *= al;
      mrun = mn;
    }
    const float mo = -mrun * SC2;
    v4u pf[2];
#pragma unroll
    for (int T = 0; T < 4; ++T) {
      float p[4];
#pragma unroll
      for (int e = 0; e < 4; ++e) p[e] = __builtin_amdgcn_exp2f(fmaf(sc[T][e], SC2, mo));
      pf[T >> 1][(T & 1) * 2] = cvtpk(p[0], p[1]);
      pf[T >> 1][(T & 1) * 2 + 1] = cvtpk(p[2], p[3]);
    }
    accl = mfma(ones, pf[0], accl);
    accl = mfma(ones, pf[1], accl);
    asm volatile("" : "+v"(pf[0]), "+v"(pf[1]), "+v"(accl) :: "memory");
    if (n == NTILE - 1) {
      float mw[3], lw[3]; f32x4 oa[3], ob[3];
#pragma unroll
      for (int w = 0; w < 3; ++w) {
        lds_wait_ge(CT + C_MRG + w, 1u);
        const LAS float* rw = (const LAS float*)(L + L_RING + w * SLOT);
        mw[w] = rw[512 + (r & 3)]; lw[w] = rw[516 + (r & 3)];
        oa[w] = *(const LAS f32x4*)(rw + (r & 3) * 128 + 32 * (r >> 2) + 4 * g); ob[w] = *(const LAS f32x4*)(rw + (r & 3) * 128 + 32 * (r >> 2) + 16 + 4 * g);
      }
      const unsigned m24 = f24(fmaxf(fmaxf(mrun, mw[0]), fmaxf(mw[1], mw[2])) * SC2);
      const float Mr = __uint_as_float(m24 << 8);
      fown = __builtin_amdgcn_exp2f(fmaf(mrun, SC2, -Mr));
      float Ls = fown * accl[0];
#pragma unroll
      for (int w = 0; w < 3; ++w) {
        const float f = __builtin_amdgcn_exp2f(fmaf(mw[w], SC2, -Mr));
        Ls = fmaf(f, lw[w], Ls); basea += f * oa[w]; baseb += f * ob[w];
      }
      const float inv = 1.f / Ls;
      fown *= inv; basea *= inv; baseb *= inv;
      if (lane < 4) st16(rs, (unsigned)W_PG + (unsigned)(kap * 4 + lane) * PGB + (unsigned)(16384 + jg * 16), v4u{m24 | TAG_LO, TAG_HI, f24(Ls) | TAG_LO, TAG_HI});
    }
    if (lap) READ_V();
#pragma unroll
    for (int ks = 0; ks < 2; ++ks)
#pragma unroll
      for (int m = 0; m < 8; ++m) acc[m] = mfma(vf[ks][m], pf[ks], acc[m]);
  }
  TS(4);
  if (c == 3) {
    const int u = r >> 2;
    const f32x4 sa = u == 0 ? acc[0] : u == 1 ? acc[2] : u == 2 ? acc[4] : acc[6], sb = u == 0 ? acc[1] : u == 1 ? acc[3] : u == 2 ? acc[5] : acc[7];
    const f32x4 xa = fown * sa + basea, xb = fown * sb + baseb;
    const unsigned po = (unsigned)W_PG + (unsigned)(kap * 4 + (r & 3)) * PGB + (unsigned)(jg * 512 + (16 * u + 2 * g) * 8);
    st16(rs, po, v4u{cvtpk(xa[0], xa[1]), T32, cvtpk(xa[2], xa[3]), T32});
    st16(rs, po + 64u, v4u{cvtpk(xb[0], xb[1]), T32, cvtpk(xb[2], xb[3]), T32});
    TS(2);
  } else {
    LAS float* rec = (LAS float*)(L + L_RING + c * SLOT);
    if (r < 4) {
#pragma unroll
      for (int m = 0; m < 8; ++m) *(LAS f32x4*)(rec + r * 128 + 16 * m + 4 * g) = acc[m];
      if (g == 0) { rec[512 + r] = mrun; rec[516 + r] = accl[0]; }
    }
    LDS_WAIT();
    CT[C_MRG + c] = 1u;
  }
  lds_wait_ge(CT + C_CLEAN, 1u);
  {
    LAS unsigned* stw = (LAS unsigned*)(L + L_RING + c * SLOT + 4096) + lane;
    gu64* pgp = (gu64*)(a.ws + W_PG + (size_t)ht * PGB + c * 4096);
    unsigned need = 0xffu, ca = 0u, cb = 0u;
    bool pre = false;
    v4u bw[4]; f32x4 dff = {0.f, 0.f, 0.f, 0.f};
    u64 va[8], vb[8];
#define H1_ISSUE(v, cm) do { cm = need & (CT[C_SEEN] >> (8 * c)); \
      if (cm) { _Pragma("unroll") for (int i = 0; i < 8; ++i) v[i] = __hip_atomic_load(((cm >> i) & 1u) ? pgp + i * 64 + lane : pgp, RLX_AGENT); } } while (0)
#define H1_CHECK(v, cm) do { if (cm) { _Pragma("unroll") for (int i = 0; i < 8; ++i) \
      if (((cm & need) >> i) & 1u) { if (__all((unsigned)(v[i] >> 32) == T32)) { stw[i * (STG_ROW / 4)] = (unsigned)v[i]; need &= ~(1u << i); } } } } while (0)
    _Pragma("unroll 1") for (unsigned sp = 0; sp < LSPIN; ++sp) {
      H1_ISSUE(va, ca);
      if (!pre && CT[C_WOK] != 0u) {
        CBAR();
        LAS unsigned char* wb = L + L_A + (8 * g + (r >> 2)) * 128 + c * 32 + (r & 3) * 8;
#pragma unroll
        for (int ks = 0; ks < 4; ++ks) { const v2u t0 = trread(wb + ks * 4096), t1 = trread(wb + ks * 4096 + 512); bw[ks] = v4u{t0.x, t0.y, t1.x, t1.y}; }
#pragma unroll
        for (int ks = 0; ks < 2; ++ks) {
          const v2u t0 = trread(wb + SLOT + ks * 4096), t1 = trread(wb + SLOT + ks * 4096 + 512);
          const v4u hf = *(const LAS v4u*)(L + L_HB + (4 * (r & 1) + (r >> 2)) * HB_ROW + 64 * ks + 16 * g);
          dff = mfma(hf, v4u{t0.x, t0.y, t1.x, t1.y}, dff);
        }
        pre = true;
      }
      H1_CHECK(vb, cb);
      if (need == 0u && pre) break;
      __builtin_amdgcn_s_sleep(H1_SLEEP);
      H1_ISSUE(vb, cb);
      H1_CHECK(va, ca);
      if (need == 0u && pre) break;
      __builtin_amdgcn_s_sleep(H1_SLEEP);
    }
    LDS_WAIT();
    CT[C_STG + c] = 1u;
#pragma unroll 1
    for (unsigned sp = 0; sp < LSPIN; ++sp) { const v4u fl = *(volatile LAS v4u*)(CT + C_STG); if (fl.x & fl.y & fl.z & fl.w) break; }
    CBAR();
    if (c == 3) TS(3);
    const LAS float* mls = (const LAS float*)(L + L_ML);
    LAS unsigned char* stg = L + L_RING + (r >> 3) * SLOT + 4096 + (r & 7) * STG_ROW + 16 * g;
#pragma unroll
    for (int rt = 0; rt < 2; ++rt) {
      f32x4 d = {0.f, 0.f, 0.f, 0.f};
#pragma unroll
      for (int ks = 0; ks < 4; ++ks) d = mfma(*(const LAS v4u*)(stg + 2 * rt * SLOT + 64 * ks), bw[ks], d);
      const f32x4 m01 = *(const LAS f32x4*)(mls + 32 * rt + 8 * g), m23 = *(const LAS f32x4*)(mls + 32 * rt + 8 * g + 4);
      const float Mx = fmaxf(fmaxf(m01[0], m01[2]), fmaxf(m23[0], m23[2]));
      const float w0 = __builtin_amdgcn_exp2f(m01[0] - Mx) * m01[1], w1 = __builtin_amdgcn_exp2f(m01[2] - Mx) * m01[3];
      const float w2 = __builtin_amdgcn_exp2f(m23[0] - Mx) * m23[1], w3 = __builtin_amdgcn_exp2f(m23[2] - Mx) * m23[3];
      const float gate = __uint_as_float(CT[C_G + 4 * rt + g]);
      const float att = ((w0 * d[0] + w1 * d[1]) + (w2 * d[2] + w3 * d[3])) / ((w0 + w1) + (w2 + w3));
      const float t = fmaf(gate, att, (1.f - gate) * dff[rt]);
      __hip_atomic_store(ws64 + W_OG / 8 + ((size_t)((4 * rt + g) * 32 + 4 * n8 + c) * 32 + ht) * 16 + r, ((u64)TAG_HI << 32) | (f24(t) | TAG_LO), RLX_AGENT);
    }
  }
  if (c == 0) TS(2);
  if (c == 0) {
    const unsigned oo = (unsigned)W_OG + (unsigned)(bid * 4096 + lane * 16);
    float t0 = 0.f, t1 = 0.f;
    v4u va[4], vb[4];
    bool ok;
#define H2_ISSUE(v) do { _Pragma("unroll") for (int i = 0; i < 4; ++i) v[i] = ld16(rs, oo + i * 1024u); } while (0)
#define H2_CHECK(v) do { t0 = 0.f; t1 = 0.f; ok = true; \
      _Pragma("unroll") for (int i = 0; i < 4; ++i) { ok &= TAG_OK(v[i].x, v[i].y) && TAG_OK(v[i].z, v[i].w); t0 += __uint_as_float(v[i].x << 8); t1 += __uint_as_float(v[i].z << 8); } } while (0)
    H2_ISSUE(va);
    _Pragma("unroll 1") for (unsigned sp = 0; sp < GSPIN; ++sp) {
      __builtin_amdgcn_s_sleep(H2_SLEEP);
      H2_ISSUE(vb);
      H2_CHECK(va);
      if (__all(ok)) break;
      __builtin_amdgcn_s_sleep(H2_SLEEP);
      H2_ISSUE(va);
      H2_CHECK(vb);
      if (__all(ok)) break;
    }
    t0 += dpp<0x128>(t0); t1 += dpp<0x128>(t1);
    t0 = xrow_sum(t0); t1 = xrow_sum(t1);
    if (lane < 8) ((GAS unsigned*)a.out)[bid * 8 + lane] = cvtpk(t0, t1);
    TS(3);
    if (bid == 0 && lane == 0) __hip_atomic_store(ws64 + W_EPOCH / 8, T64, RLX_AGENT);
  }
  TS_FLUSH(wave);
}
}

extern "C" void kernel_launch(void* const* d_in, const int* in_sizes, int n_in, void* d_out, int out_size, void* d_ws,
                              size_t ws_size, hipStream_t stream) {
  (void)in_sizes; (void)n_in; (void)out_size;
  if (ws_size < W_END) return;
  Args a;
  a.x = (const unsigned char*)d_in[0]; a.k = (const unsigned char*)d_in[1]; a.v = (const unsigned char*)d_in[2];
  a.wq = (const unsigned char*)d_in[3]; a.wo = (const unsigned char*)d_in[4];
  a.wg = (const unsigned char*)d_in[6]; a.wu = (const unsigned char*)d_in[7]; a.wd = (const unsigned char*)d_in[8];
  a.gw = (const unsigned char*)d_in[9];
  a.out = (unsigned char*)d_out; a.ws = (unsigned char*)d_ws;
  (void)hipFuncSetAttribute((const void*)fused_kernel, hipFuncAttributeMaxDynamicSharedMemorySize, L_BYTES);
  hipLaunchKernelGGL(fused_kernel, dim3(NWG), dim3(NTHR), L_BYTES, stream, a);
}
```

```cpp
#define H1_DIRECT 16
#define H1_SLEEP 3
#define H2_SLEEP 3
#define ML_SLEEP 4
#define PACE_R 540
#define PACE_A 90
#include <hip/hip_runtime.h>
#include <hip/hip_bf16.h>
#include <stdint.h>

#define GAS __attribute__((address_space(1)))
#define LAS __attribute__((address_space(3)))
typedef unsigned v4u __attribute__((ext_vector_type(4)));
typedef unsigned v2u __attribute__((ext_vector_type(2)));
typedef float f32x4 __attribute__((ext_vector_type(4)));
typedef float f32x2 __attribute__((ext_vector_type(2)));
typedef __bf16 bf16x8 __attribute__((ext_vector_type(8)));
typedef short s16x4 __attribute__((ext_vector_type(4)));
typedef unsigned long long u64;
typedef GAS u64 gu64;
#define RLX_AGENT __ATOMIC_RELAXED, __HIP_MEMORY_SCOPE_AGENT

namespace {
constexpr int NB = 8, DM = 512, HQ = 32, HKV = 8, SK = 8192, DH = 128, FF = 1792;
constexpr int NWG = 256, NTHR = 6 * 64;
constexpr int NTILE = 32;
#define SW 16
#define WO_AT 32
#define TPAT 4
#define QG_AT 6u
constexpr int SLOT = 16384;
constexpr int L_RING = 0, L_A = 8 * SLOT, L_B = L_A + SLOT, L_Q = L_B + 8192, L_HB = L_Q + 1024, L_ML = L_HB + 2048, L_CTRL = L_ML + 256, L_BYTES = L_CTRL + 256;
constexpr int STG_ROW = 272;
constexpr int HB_ROW = 144;
constexpr int C_LANDED = 0, C_GUFREE = 1, C_QRDY = 2, C_SEEN = 3, C_DONE = 4, C_MRG = 8, C_STG = 12, C_TAG = 16, C_TAGOK = 18, C_WOK = 19, C_CLEAN = 20, C_G = 24;
static_assert(L_BYTES <= 160 * 1024, "LDS");
constexpr size_t W_EPOCH = 0, W_G = 256, W_FLAG = 4096;
constexpr size_t W_QG = 8192;
constexpr size_t W_GP = W_QG + (size_t)2 * NB * 4096 * 8;
constexpr size_t W_HG = W_GP + (size_t)224 * 2048;
constexpr size_t W_PG = W_HG + (size_t)NB * FF * 8;
constexpr unsigned PGB = 16384 + 512;
constexpr size_t W_OG = W_PG + (size_t)HQ * PGB;
constexpr size_t W_END = W_OG + (size_t)NWG * 4096;
static_assert(W_END < (1u << 30) && W_PG % 1024 == 0, "buffer offsets are int");
constexpr u64 EPOCH_STEP = 0x9E3779B97F4A7C15ull;
constexpr unsigned GSPIN = 1u << 15, LSPIN = 1u << 18;
constexpr float SC2 = 0.08838834764831845f * 1.4426950408889634f;

struct Args {
  const unsigned char *x, *k, *v, *wq, *wo, *wg, *wu, *wd, *gw;
  unsigned char *out, *ws;
};

__device__ __forceinline__ f32x4 mfma(v4u a, v4u b, f32x4 c) {
  return __builtin_amdgcn_mfma_f32_16x16x32_bf16(__builtin_bit_cast(bf16x8, a), __builtin_bit_cast(bf16x8, b), c, 0, 0, 0);
}
__device__ __forceinline__ v2u trread(LAS unsigned char* p) {
  return __builtin_bit_cast(v2u, __builtin_amdgcn_ds_read_tr16_b64_v4i16((LAS s16x4*)p));
}
__device__ __forceinline__ unsigned cvtpk(float lo, float hi) {
  typedef __bf16 bf16x2 __attribute__((ext_vector_type(2)));
  bf16x2 r; r[0] = (__bf16)lo; r[1] = (__bf16)hi; return __builtin_bit_cast(unsigned, r);
}
template <int CTRL> __device__ __forceinline__ float dpp(float x) {
  return __builtin_bit_cast(float, __builtin_amdgcn_mov_dpp(__builtin_bit_cast(int, x), CTRL, 0xf, 0xf, true));
}
__device__ __forceinline__ float xrow_max(float x) {
  auto s = __builtin_amdgcn_permlane16_swap(__float_as_uint(x), __float_as_uint(x), false, false);
  x = fmaxf(__uint_as_float(s[0]), __uint_as_float(s[1]));
  auto t = __builtin_amdgcn_permlane32_swap(__float_as_uint(x), __float_as_uint(x), false, false);
  return fmaxf(__uint_as_float(t[0]), __uint_as_float(t[1]));
}
__device__ __forceinline__ float xrow_sum(float x) {
  auto s = __builtin_amdgcn_permlane16_swap(__float_as_uint(x), __float_as_uint(x), false, false);
  x = __uint_as_float(s[0]) + __uint_as_float(s[1]);
  auto t = __builtin_amdgcn_permlane32_swap(__float_as_uint(x), __float_as_uint(x), false, false);
  return __uint_as_float(t[0]) + __uint_as_float(t[1]);
}
__device__ __forceinline__ float bflo(unsigned u) { return __uint_as_float(u << 16); }
__device__ __forceinline__ float bfhi(unsigned u) { return __uint_as_float(u & 0xffff0000u); }

__device__ __forceinline__ v4u ld16(__amdgpu_buffer_rsrc_t rs, unsigned off) { return __builtin_amdgcn_raw_buffer_load_b128(rs, (int)off, 0, 16); }
__device__ __forceinline__ void st16(__amdgpu_buffer_rsrc_t rs, unsigned off, v4u v) { __builtin_amdgcn_raw_buffer_store_b128(v, rs, (int)off, 0, 16); }
__device__ __forceinline__ unsigned f24(float v) { return (__float_as_uint(v) + 0x80u) >> 8; }
#define TAG_LO ((unsigned)T64 << 24)
#define TAG_HI ((unsigned)(T64 >> 8))
#define TAG_OK(lo, hi) ((hi) == TAG_HI && ((lo) ^ TAG_LO) < 0x1000000u)
#define CBAR() asm volatile("" ::: "memory")
#define LDS_WAIT() asm volatile("s_waitcnt lgkmcnt(0)" ::: "memory")
#define KPOL " nt"
#define VPOL " nt"
#define WPOL " nt"
#define DMA4(sbase, dst, v0, v1, v2, v3, POL)                                                                    \
  asm volatile("s_mov_b32 m0, %5\n\ts_nop 0\n\t"                                                                 \
               "global_load_lds_dwordx4 %0, %4" POL "\n\t"                                                       \
               "global_load_lds_dwordx4 %1, %4 offset:1024" POL "\n\t"                                           \
               "global_load_lds_dwordx4 %2, %4 offset:2048" POL "\n\t"                                           \
               "global_load_lds_dwordx4 %3, %4 offset:3072" POL                                                  \
               :: "v"(v0), "v"(v1), "v"(v2), "v"(v3), "s"(sbase), "s"(dst) : "memory")
#define DMA1S(sbase, voff, dst)                                                                                  \
  asm volatile("s_mov_b32 m0, %2\n\ts_nop 0\n\tglobal_load_lds_dwordx4 %0, %1" WPOL :: "v"(voff), "s"(sbase), "s"(dst) : "memory")
#define DMA1V(vaddr, dst)                                                                                        \
  asm volatile("s_mov_b32 m0, %1\n\ts_nop 0\n\tglobal_load_lds_dwordx4 %0, off" WPOL :: "v"(vaddr), "s"(dst) : "memory")
#define VMWAIT(n) asm volatile("s_waitcnt vmcnt(" #n ")" ::: "memory")
#define VMWAITN(n) asm volatile("s_waitcnt vmcnt(%0)" :: "i"(n) : "memory")
#define TS(i) do { } while (0)
#define TS_FLUSH(w) do { } while (0)

__device__ __forceinline__ void lds_wait_ge(volatile LAS unsigned* w, unsigned need) {
#pragma unroll 1
  for (unsigned sp = 0; *w < need && sp < LSPIN; ++sp) __builtin_amdgcn_s_sleep(1);
  CBAR();
}

__global__ void __launch_bounds__(NTHR, 2) fused_kernel(Args a) {
  extern __shared__ __attribute__((aligned(16))) unsigned char smem[];
  LAS unsigned char* L = (LAS unsigned char*)smem;
  volatile LAS unsigned* CT = (volatile LAS unsigned*)(L + L_CTRL);
  const int tid = threadIdx.x, lane = tid & 63, wave = __builtin_amdgcn_readfirstlane(tid >> 6);
  const int bid = blockIdx.x;
  const int kap = bid >> 5, jg = bid & 31, b = jg >> 2, s = jg & 3;
  const int qcol = kap * 512 + (((jg & 7) << 1) | ((jg >> 3) & 1)) * 32, qk = jg >> 4;
  const bool has_ffn = (bid >> 3) < 28;
  const int fi = has_ffn ? (bid >> 3) : 27;
  const int fcg = (bid & 7) * 7 + (fi >> 2), fk = fi & 3;
  const int hh = jg >> 3, ht = kap * 4 + hh, n8 = jg & 7;
  const int g = lane >> 4, r = lane & 15;
  gu64* ws64 = (gu64*)a.ws;
  const __amdgpu_buffer_rsrc_t rs = __builtin_amdgcn_make_buffer_rsrc((void*)a.ws, 0, (int)W_END, 0x00020000);
  if (__builtin_expect(wave == 0, 1)) {
    __builtin_amdgcn_s_waitcnt(0);
    __builtin_amdgcn_s_setprio(3);
    const unsigned lb = (unsigned)(size_t)L;
    {
      const unsigned voff = (unsigned)(lane >> 2) * 8192u + (unsigned)(lane & 3) * 16u;
      const unsigned char* sb = a.wq + (size_t)qk * 256 * 8192 + (size_t)qcol * 2;
#pragma unroll 1
      for (int j = 0; j < 16; ++j) DMA1S(sb + (size_t)j * 16 * 8192, voff, lb + L_A + j * 1024);
    }
    {
      const unsigned char* p = ((lane & 32) ? a.wu : a.wg) + (size_t)(fk * 128 + ((lane >> 2) & 7)) * (FF * 2) + (size_t)fcg * 64 + (lane & 3) * 16;
#pragma unroll 1
      for (int j = 0; j < 16; ++j) DMA1V(p + (size_t)j * 8 * (FF * 2), lb + L_RING + 7 * SLOT + j * 1024);
    }
    __builtin_amdgcn_s_barrier();
    CBAR();
    unsigned ko[4], vo[2];
#pragma unroll
    for (int j = 0; j < 4; ++j) ko[j] = (unsigned)g * 256u + (unsigned)((r ^ g ^ (4 * j)) * 16);
#pragma unroll
    for (int j = 0; j < 2; ++j) vo[j] = (unsigned)g * 256u + (unsigned)((r ^ (2 * (4 * j + g))) * 16);
    const size_t seq_off = (size_t)(b * HKV + kap) * SK * 256;
    const unsigned char* kb = a.k + seq_off;
    const unsigned char* vb = a.v + seq_off;
#define LOAD_WO(last) do { \
      const unsigned voff = (unsigned)(lane >> 3) * 1024u + (unsigned)(lane & 7) * 16u; \
      const unsigned char* sb = a.wo + (size_t)ht * 128 * 1024 + (size_t)n8 * 128; \
      const unsigned char* sd = a.wd + (size_t)ht * 56 * 1024 + (size_t)n8 * 128; \
      _Pragma("unroll 1") for (int j = 0; j < 23; ++j) { \
        DMA1S(j < 16 ? sb + (size_t)j * 8192 : sd + (size_t)(j - 16) * 8192, voff, lb + L_A + j * 1024); \
        if (last) { if (j == 7) { VMWAIT(8); CT[C_LANDED] = 2u + 2u * NTILE; } } \
        else if ((j & 7) == 7) VMWAIT(16); \
      } } while (0)
    unsigned seen = 0;
    const unsigned pace0 = (unsigned)__builtin_amdgcn_s_memrealtime();
    bool deep = true;
#pragma unroll 1
    for (int n = 0; n < NTILE; ++n) {
      const int tile = ((n + 4 * b) & 31) * 4 + s;
      const int cs = n & 3;
      if (n == WO_AT) LOAD_WO(false);
      if (n == WO_AT + 2) CT[C_WOK] = 1u;
#pragma unroll
      for (int hv = 0; hv < 2; ++hv) {
        const unsigned f = 2u + 2u * n + hv;
        if (n >= 4 || (n == 3 && hv == 1)) {
          volatile LAS unsigned* fw = n >= 4 ? (CT + C_DONE + cs) : (CT + C_GUFREE);
          const unsigned need = n >= 4 ? (unsigned)(2 * (n >> 2) - 1 + hv) : 1u;
          if (n < 4 || seen < need) {
            if (*fw < need) {
              VMWAIT(0); CT[C_LANDED] = f;
              lds_wait_ge(fw, need);
            }
          }
        }
        const unsigned char* sb = (hv ? vb : kb) + (size_t)tile * SLOT;
        const unsigned dst = lb + L_RING + (hv * 4 + cs) * SLOT;
        if (hv == 0) { DMA4(sb, dst, ko[0], ko[1], ko[2], ko[3], KPOL); DMA4(sb + 4096, dst + 4096, ko[0], ko[1], ko[2], ko[3], KPOL); }
        else         { DMA4(sb, dst, vo[0], vo[1], vo[0], vo[1], VPOL); DMA4(sb + 4096, dst + 4096, vo[0], vo[1], vo[0], vo[1], VPOL); }
        if (deep) VMWAIT(32); else VMWAIT(16);
        if (hv == 0) { DMA4(sb + 8192, dst + 8192, ko[0], ko[1], ko[2], ko[3], KPOL); DMA4(sb + 12288, dst + 12288, ko[0], ko[1], ko[2], ko[3], KPOL); }
        else         { DMA4(sb + 8192, dst + 8192, vo[0], vo[1], vo[0], vo[1], VPOL); DMA4(sb + 12288, dst + 12288, vo[0], vo[1], vo[0], vo[1], VPOL); }
        const unsigned nx = CT[C_DONE + (hv ? ((cs + 1) & 3) : cs)];
        const unsigned now = (unsigned)__builtin_amdgcn_s_memrealtime() - pace0;
        if (deep) { VMWAIT(32); CT[C_LANDED] = f - 1; } else { VMWAIT(16); CT[C_LANDED] = f; }
        deep = now > (unsigned)(PACE_A + PACE_R * (int)f / 10 + (n >= WO_AT ? 80 : 0)) && f < 2u * NTILE;
        seen = __builtin_amdgcn_readfirstlane(nx);
        if (f == 12) TS(1);
        if (f == 3) TS(5);
        if (f == 6) TS(6);
        if (f == 34) TS(7);
      }
    }
    {
      if (WO_AT < NTILE) {
        VMWAIT(16); CT[C_LANDED] = 1u + 2u * NTILE;
        VMWAIT(0); CT[C_LANDED] = 2u + 2u * NTILE;
        TS(2);
      } else {
        VMWAIT(16); CT[C_LANDED] = 1u + 2u * NTILE;
        LOAD_WO(true);
        TS(2);
        VMWAIT(0);
      }
      CT[C_WOK] = 1u;
      TS(3);
      TS_FLUSH(0);
    }
    return;
  }

  const int c = wave - 1;
  u64 T64 = 0;
  v4u xf[8];
  if (wave == 5) T64 = __hip_atomic_load(ws64 + W_EPOCH / 8, RLX_AGENT);
  if (wave <= 2) {
    const GAS v4u* xp = (const GAS v4u*)(a.x + (size_t)(r & 7) * 1024 + (wave == 1 ? qk * 512 : fk * 256) + g * 16);
#pragma unroll
    for (int ks = 0; ks < 8; ++ks) xf[ks] = xp[(wave == 1 ? ks : (ks & 3)) * 4];
  }
  if (wave == 3) ((LAS unsigned*)(L + L_CTRL))[lane] = 0u;
  if (wave == 4) ((LAS v4u*)(L + L_B + 7168))[lane] = v4u{0u, 0u, 0u, 0u};
  LDS_WAIT();
  __builtin_amdgcn_s_barrier();
  CBAR();

  if (wave == 5) {
    T64 += EPOCH_STEP;
    if (lane == 0) { CT[C_TAG] = (unsigned)T64; CT[C_TAG + 1] = (unsigned)(T64 >> 32); LDS_WAIT(); CT[C_TAGOK] = 1u; }
    if (bid == NWG - 1) {
      const int gb = lane >> 3, part = lane & 7;
      const GAS v4u* xp = (const GAS v4u*)(a.x + (size_t)gb * 1024 + part * 128);
      const GAS v4u* gp = (const GAS v4u*)(a.gw + part * 128);
      float ga = 0.f;
#pragma unroll
      for (int i = 0; i < 8; ++i) {
        const v4u xv = xp[i], gv = gp[i];
#pragma unroll
        for (int e = 0; e < 4; ++e) { ga = fmaf(bflo(xv[e]), bflo(gv[e]), ga); ga = fmaf(bfhi(xv[e]), bfhi(gv[e]), ga); }
      }
      ga += dpp<0xB1>(ga); ga += dpp<0x4E>(ga); ga += dpp<0x141>(ga);
      const float gate = 1.f / (1.f + __expf(-ga));
      if (part == 0) __hip_atomic_store(ws64 + W_G / 8 + gb, ((u64)TAG_HI << 32) | (f24(gate) | TAG_LO), RLX_AGENT);
    }
    {
      lds_wait_ge(CT + C_LANDED, QG_AT);
      const unsigned qo = (unsigned)W_QG + (unsigned)((b * HKV + kap) * 8192 + lane * 16);
      _Pragma("unroll 1") for (unsigned sp = 0; sp < GSPIN; ++sp) {
        v4u v[8]; bool ok = true;
#pragma unroll
        for (int i = 0; i < 8; ++i) v[i] = ld16(rs, qo + i * 1024u);
#pragma unroll
        for (int i = 0; i < 4; ++i) {
          ok &= TAG_OK(v[i].x, v[i].y) && TAG_OK(v[i].z, v[i].w) && TAG_OK(v[i + 4].x, v[i + 4].y) && TAG_OK(v[i + 4].z, v[i + 4].w);
          ((LAS unsigned*)(L + L_Q))[i * 64 + lane] = cvtpk(__uint_as_float(v[i].x << 8) + __uint_as_float(v[i + 4].x << 8), __uint_as_float(v[i].z << 8) + __uint_as_float(v[i + 4].z << 8));
        }
        if (__all(ok)) break;
        __builtin_amdgcn_s_sleep(8);
      }
      LDS_WAIT(); CT[C_QRDY] = 1u;
      TS(2);
    }
    {
      lds_wait_ge(CT + C_LANDED, 12u);
      const int fb = lane < 32 ? kap * 32 + lane : (bid & 7) + 8 * ((fi & ~3) + (lane & 3));
      _Pragma("unroll 1") for (unsigned sp = 0; sp < GSPIN; ++sp) {
        if (__all(__hip_atomic_load(ws64 + W_FLAG / 8 + fb, RLX_AGENT) == T64)) break;
        __builtin_amdgcn_s_sleep(32);
      }
      CBAR();
    }
    if (has_ffn) {
      const unsigned go = (unsigned)W_GP + (unsigned)((fcg * 4 + fk) * 2048 + lane * 16);
      const f32x4 p0 = __builtin_bit_cast(f32x4, ld16(rs, go)), p1 = __builtin_bit_cast(f32x4, ld16(rs, go + 1024u));
      unsigned hw[4];
#pragma unroll
      for (int e = 0; e < 4; ++e) {
        const unsigned t = __float_as_uint(p0[e] + p1[e]);
        auto s32 = __builtin_amdgcn_permlane32_swap(t, t, false, false);
        const unsigned tot = __float_as_uint(__uint_as_float(s32[0]) + __uint_as_float(s32[1]));
        auto s16 = __builtin_amdgcn_permlane16_swap(tot, tot, false, false);
        const float gt = __uint_as_float(s16[0]), up = __uint_as_float(s16[1]);
        const float u = 0.7978845608028654f * (gt + 0.044715f * gt * gt * gt);
        const float th = 1.f - 2.f / (__expf(2.f * u) + 1.f);
        hw[e] = f24(0.5f * gt * (1.f + th) * up) | TAG_LO;
      }
      const int src = ((2 * (g & 1) + (r >> 3)) * 4 + ((r & 7) >> 1)) * 4;
      const unsigned a0 = __builtin_amdgcn_ds_bpermute(src, (int)hw[0]), a1 = __builtin_amdgcn_ds_bpermute(src, (int)hw[1]);
      const unsigned a2 = __builtin_amdgcn_ds_bpermute(src, (int)hw[2]), a3 = __builtin_amdgcn_ds_bpermute(src, (int)hw[3]);
      if (lane < 32)
        st16(rs, (unsigned)W_HG + (unsigned)((4 * (fk & 1) + 2 * g + (r >> 3)) * FF + fcg * 32 + (fk >> 1) * 16 + 2 * (r & 7)) * 8u,
             v4u{(r & 1) ? a2 : a0, TAG_HI, (r & 1) ? a3 : a1, TAG_HI});
    }
    {
      lds_wait_ge(CT + C_LANDED, 20u);
      const bool pad = (lane & 31) >= 28;
      const unsigned ho = (unsigned)W_HG + (unsigned)((lane >> 5) * FF + ht * 56 + (pad ? 0 : 2 * (lane & 31))) * 8u;
      _Pragma("unroll 1") for (unsigned sp = 0; sp < GSPIN; ++sp) {
        v4u v[4];
#pragma unroll
        for (int i = 0; i < 4; ++i) v[i] = ld16(rs, ho + (unsigned)(i * 2 * FF * 8));
        const u64 gv = __hip_atomic_load(ws64 + W_G / 8 + (lane & 7), RLX_AGENT);
        bool ok = TAG_OK((unsigned)gv, (unsigned)(gv >> 32));
#pragma unroll
        for (int i = 0; i < 4; ++i) {
          ok &= TAG_OK(v[i].x, v[i].y) && TAG_OK(v[i].z, v[i].w);
          *(LAS unsigned*)(L + L_HB + (2 * i + (lane >> 5)) * HB_ROW + (lane & 31) * 4) =
              pad ? 0u : cvtpk(__uint_as_float(v[i].x << 8), __uint_as_float(v[i].z << 8));
        }
        if (lane < 8) CT[C_G + lane] = (unsigned)gv << 8;
        if (__all(ok)) break;
        __builtin_amdgcn_s_sleep(32);
      }
      LDS_WAIT();
      CT[C_CLEAN] = 1u;
      TS(1);
    }
    {
      lds_wait_ge(CT + C_LANDED, 2u * NTILE - 2u);
      gu64* mlp = (gu64*)(a.ws + W_PG + (size_t)ht * PGB + 16384) + lane;
      u64 ma = __hip_atomic_load(mlp, RLX_AGENT), mb;
#define ML_CHECK(v) do { const bool mk = TAG_OK((unsigned)v, (unsigned)(v >> 32)); if (mk) ((LAS unsigned*)(L + L_ML))[lane] = (unsigned)v << 8; \
        u64 k = __ballot(mk); k &= k >> 1; k &= 0x5555555555555555ull; \
        k = (k | (k >> 1)) & 0x3333333333333333ull; k = (k | (k >> 2)) & 0x0f0f0f0f0f0f0f0full; k = (k | (k >> 4)) & 0x00ff00ff00ff00ffull; \
        k = (k | (k >> 8)) & 0x0000ffff0000ffffull; k = (k | (k >> 16)) & 0xffffffffull; \
        sn = (unsigned)k; LDS_WAIT(); CT[C_SEEN] = sn; } while (0)
      unsigned sn;
      _Pragma("unroll 1") for (unsigned sp = 0; sp < GSPIN; ++sp) {
        __builtin_amdgcn_s_sleep(ML_SLEEP);
        mb = __hip_atomic_load(mlp, RLX_AGENT);
        ML_CHECK(ma);
        if (sn == ~0u) break;
        __builtin_amdgcn_s_sleep(ML_SLEEP);
        ma = __hip_atomic_load(mlp, RLX_AGENT);
        ML_CHECK(mb);
        if (sn == ~0u) break;
      }
    }
    TS(3);
    TS_FLUSH(5);
    return;
  }

  if (c == 0) {
    lds_wait_ge(CT + C_TAGOK, 1u);
    T64 = ((u64)CT[C_TAG + 1] << 32) | CT[C_TAG];
    lds_wait_ge(CT + C_LANDED, 1u);
    TS(0);
    LAS unsigned char* sl = L + L_A + (8 * g + (r >> 2)) * 64 + (r & 3) * 8;
    f32x4 d[2] = {{0.f, 0.f, 0.f, 0.f}, {0.f, 0.f, 0.f, 0.f}};
#pragma unroll
    for (int ks = 0; ks < 8; ++ks)
#pragma unroll
      for (int ct = 0; ct < 2; ++ct) {
        const v2u b0 = trread(sl + ks * 2048 + ct * 32), b1 = trread(sl + ks * 2048 + ct * 32 + 256);
        d[ct] = mfma(xf[ks], v4u{b0.x, b0.y, b1.x, b1.y}, d[ct]);
      }
    const f32x4 dv = g < 2 ? d[0] : d[1];
    gu64* qg = ws64 + W_QG / 8 + (size_t)((4 * (g & 1) * HKV + kap) * 2 + qk) * 512 + (qcol & 511) + (g >> 1) * 16 + r;
#pragma unroll
    for (int e = 0; e < 4; ++e) __hip_atomic_store(qg + (size_t)e * HKV * 1024, ((u64)TAG_HI << 32) | (f24(dv[e]) | TAG_LO), RLX_AGENT);
    TS(1);
  } else if (c == 1) {
    lds_wait_ge(CT + C_TAGOK, 1u);
    T64 = ((u64)CT[C_TAG + 1] << 32) | CT[C_TAG];
    const v4u inv = {0u, ~(unsigned)T64, 0u, ~(unsigned)T64};
    const unsigned co = (unsigned)W_PG + (unsigned)(kap * 4 + (lane >> 5)) * PGB + (unsigned)(jg * 512 + (lane & 31) * 16);
    st16(rs, co, inv);
    st16(rs, co + 2u * PGB, inv);
    lds_wait_ge(CT + C_LANDED, 2u);
    LAS unsigned char* sl = L + L_RING + 7 * SLOT + g * 1024 + (r >> 2) * 64 + (r & 3) * 8;
    f32x4 d[4] = {{0.f, 0.f, 0.f, 0.f}, {0.f, 0.f, 0.f, 0.f}, {0.f, 0.f, 0.f, 0.f}, {0.f, 0.f, 0.f, 0.f}};
#pragma unroll
    for (int ks = 0; ks < 4; ++ks)
#pragma unroll
      for (int mc = 0; mc < 4; ++mc) {
        LAS unsigned char* q = sl + ks * 4096 + (mc >> 1) * 512 + (mc & 1) * 32;
        const v2u b0 = trread(q), b1 = trread(q + 256);
        d[mc] = mfma(xf[ks], v4u{b0.x, b0.y, b1.x, b1.y}, d[mc]);
      }
    LDS_WAIT();
    CT[C_GUFREE] = 1u;
    if (has_ffn) {
      const f32x4 dg = g < 2 ? d[0] : d[1], du = g < 2 ? d[2] : d[3];
      GAS unsigned* gp = (GAS unsigned*)(a.ws + W_GP + (size_t)(fcg * 4 + 2 * (g >> 1) + (g & 1)) * 2048 + fk * 512) + r;
#pragma unroll
      for (int e = 0; e < 4; ++e) {
        __hip_atomic_store(gp + e * 16, __float_as_uint(dg[e]), RLX_AGENT);
        __hip_atomic_store(gp + 64 + e * 16, __float_as_uint(du[e]), RLX_AGENT);
      }
    }
    VMWAIT(0);
    if (lane == 0) __hip_atomic_store(ws64 + W_FLAG / 8 + bid, T64, RLX_AGENT);
  } else {
    lds_wait_ge(CT + C_TAGOK, 1u);
    T64 = ((u64)CT[C_TAG + 1] << 32) | CT[C_TAG];
  }
  const unsigned T32 = (unsigned)T64;
  const v4u ones = {0x3F803F80u, 0x3F803F80u, 0x3F803F80u, 0x3F803F80u};
  LAS unsigned char* kbase = L + L_RING + c * SLOT + r * 256;
  LAS unsigned char* vbase = L + L_RING + (4 + c) * SLOT + (4 * g + (r >> 2)) * 256 + (r & 3) * 8;
  const int vx = 4 * (g & 1) + (r >> 2);

  v4u qf[4];
  f32x4 acc[8], accl = {0.f, 0.f, 0.f, 0.f};
#pragma unroll
  for (int m = 0; m < 8; ++m) acc[m] = f32x4{0.f, 0.f, 0.f, 0.f};
  float mrun = -INFINITY, fown = 0.f;
  f32x4 basea = {0.f, 0.f, 0.f, 0.f}, baseb = {0.f, 0.f, 0.f, 0.f};
#pragma unroll 1
  for (int lap = 0; lap < NTILE / 4; ++lap) {
    const unsigned n = 4u * lap + c;
    lds_wait_ge(CT + C_LANDED, 3u + 2u * n);
    v4u kf[4][4];
#pragma unroll
    for (int T = 0; T < 4; ++T)
#pragma unroll
      for (int ks = 0; ks < 4; ++ks) kf[T][ks] = *(const LAS v4u*)(kbase + T * 4096 + (((4 * ks + g) ^ r) * 16));
    LDS_WAIT();
    CT[C_DONE + c] = (unsigned)(2 * lap + 1);
#pragma unroll
    for (int T = 0; T < 4; ++T) asm volatile("" : "+v"(kf[T][0]), "+v"(kf[T][1]), "+v"(kf[T][2]), "+v"(kf[T][3]) :: "memory");
    v4u vf[2][8];
#define READ_V() do { \
      lds_wait_ge(CT + C_LANDED, 4u + 2u * n); \
      _Pragma("unroll") for (int ks = 0; ks < 2; ++ks) \
        _Pragma("unroll") for (int m = 0; m < 8; ++m) { \
          LAS unsigned char* p = vbase + ks * 8192 + ((m ^ vx) * 32); \
          const v2u t0 = trread(p), t1 = trread(p + 4096); \
          vf[ks][m] = v4u{t0.x, t0.y, t1.x, t1.y}; \
        } \
      LDS_WAIT(); \
      CT[C_DONE + c] = (unsigned)(2 * lap + 2); \
      _Pragma("unroll") for (int ks = 0; ks < 2; ++ks) { \
        asm volatile("" : "+v"(vf[ks][0]), "+v"(vf[ks][1]), "+v"(vf[ks][2]), "+v"(vf[ks][3]) :: "memory"); \
        asm volatile("" : "+v"(vf[ks][4]), "+v"(vf[ks][5]), "+v"(vf[ks][6]), "+v"(vf[ks][7]) :: "memory"); \
      } } while (0)
    if (lap == 0) {
      READ_V();
      lds_wait_ge(CT + C_QRDY, 1u);
#pragma unroll
      for (int ks = 0; ks < 4; ++ks) qf[ks] = *(const LAS v4u*)(L + L_Q + ((r >> 2) * 128 + 32 * ks + 8 * g) * 2);
    }
    f32x4 sc[4];
#pragma unroll
    for (int T = 0; T < 4; ++T) {
      f32x4 d = {0.f, 0.f, 0.f, 0.f};
#pragma unroll
      for (int ks = 0; ks < 4; ++ks) d = mfma(kf[T][ks], qf[ks], d);
      sc[T] = d;
    }
    float tm = fmaxf(fmaxf(sc[0][0], sc[0][1]), fmaxf(sc[0][2], sc[0][3]));
#pragma unroll
    for (int T = 1; T < 4; ++T) tm = fmaxf(tm, fmaxf(fmaxf(sc[T][0], sc[T][1]), fmaxf(sc[T][2], sc[T][3])));
    tm = xrow_max(tm);
    if (__any(tm > mrun)) {
      const float mn = fmaxf(mrun, tm);
      const float al = __builtin_amdgcn_exp2f((mrun - mn) * SC2);
#pragma unroll
      for (int m = 0; m < 8; ++m) acc[m] *= al;
      accl *= al;
      mrun = mn;
    }
    const float mo = -mrun * SC2;
    v4u pf[2];
#pragma unroll
    for (int T = 0; T < 4; ++T) {
      float p[4];
#pragma unroll
      for (int e = 0; e < 4; ++e) p[e] = __builtin_amdgcn_exp2f(fmaf(sc[T][e], SC2, mo));
      pf[T >> 1][(T & 1) * 2] = cvtpk(p[0], p[1]);
      pf[T >> 1][(T & 1) * 2 + 1] = cvtpk(p[2], p[3]);
    }
    accl = mfma(ones, pf[0], accl);
    accl = mfma(ones, pf[1], accl);
    asm volatile("" : "+v"(pf[0]), "+v"(pf[1]), "+v"(accl) :: "memory");
    if (n == NTILE - 1) {
      float mw[3], lw[3]; f32x4 oa[3], ob[3];
#pragma unroll
      for (int w = 0; w < 3; ++w) {
        lds_wait_ge(CT + C_MRG + w, 1u);
        const LAS float* rw = (const LAS float*)(L + L_RING + w * SLOT);
        mw[w] = rw[512 + (r >> 2)]; lw[w] = rw[516 + (r >> 2)];
        oa[w] = *(const LAS f32x4*)(rw + r * 32 + 4 * g); ob[w] = *(const LAS f32x4*)(rw + r * 32 + 16 + 4 * g);
      }
      const unsigned m24 = f24(fmaxf(fmaxf(mrun, mw[0]), fmaxf(mw[1], mw[2])) * SC2);
      const float Mr = __uint_as_float(m24 << 8);
      fown = __builtin_amdgcn_exp2f(fmaf(mrun, SC2, -Mr));
      float Ls = fown * accl[0];
#pragma unroll
      for (int w = 0; w < 3; ++w) {
        const float f = __builtin_amdgcn_exp2f(fmaf(mw[w], SC2, -Mr));
        Ls = fmaf(f, lw[w], Ls); basea += f * oa[w]; baseb += f * ob[w];
      }
      const float inv = 1.f / Ls;
      fown *= inv; basea *= inv; baseb *= inv;
      if ((lane & 51) == 0) st16(rs, (unsigned)W_PG + (unsigned)(kap * 4 + (r >> 2)) * PGB + (unsigned)(16384 + jg * 16), v4u{m24 | TAG_LO, TAG_HI, f24(Ls) | TAG_LO, TAG_HI});
    }
    if (lap) READ_V();
#pragma unroll
    for (int ks = 0; ks < 2; ++ks)
#pragma unroll
      for (int m = 0; m < 8; ++m) acc[m] = mfma(vf[ks][m], pf[ks], acc[m]);
  }
  TS(4);
  if (c == 3) {
    const int u = r & 3;
    const f32x4 sa = u == 0 ? acc[0] : u == 1 ? acc[2] : u == 2 ? acc[4] : acc[6], sb = u == 0 ? acc[1] : u == 1 ? acc[3] : u == 2 ? acc[5] : acc[7];
    const f32x4 xa = fown * sa + basea, xb = fown * sb + baseb;
    const unsigned po = (unsigned)W_PG + (unsigned)(kap * 4 + (r >> 2)) * PGB + (unsigned)(jg * 512 + g * 64 + u * 16);
    st16(rs, po, v4u{cvtpk(xa[0], xa[1]), T32, cvtpk(xa[2], xa[3]), T32});
    st16(rs, po + 256u, v4u{cvtpk(xb[0], xb[1]), T32, cvtpk(xb[2], xb[3]), T32});
    TS(2);
  } else {
    LAS float* rec = (LAS float*)(L + L_RING + c * SLOT);
    if ((r & 3) == 0) {
#pragma unroll
      for (int m = 0; m < 8; ++m) *(LAS f32x4*)(rec + (r >> 2) * 128 + 16 * m + 4 * g) = acc[m];
      if (g == 0) { rec[512 + (r >> 2)] = mrun; rec[516 + (r >> 2)] = accl[0]; }
    }
    LDS_WAIT();
    CT[C_MRG + c] = 1u;
  }
  lds_wait_ge(CT + C_CLEAN, 1u);
  {
    LAS unsigned* stw = (LAS unsigned*)(L + L_RING + c * SLOT + 4096) + 16 * ((lane >> 1) & 3) + 8 * (lane >> 5) + 2 * ((lane >> 3) & 3) + (lane & 1);
    gu64* pgp = (gu64*)(a.ws + W_PG + (size_t)ht * PGB + c * 4096);
    unsigned need = 0xffu, ca = 0u, cb = 0u;
    bool pre = false;
    v4u bw[4]; f32x4 dff = {0.f, 0.f, 0.f, 0.f};
    u64 va[8], vb[8];
#define H1_ISSUE(v, cm) do { const unsigned sn_ = CT[C_SEEN]; cm = __builtin_popcount(sn_) >= 32 - H1_DIRECT ? need : need & (sn_ >> (8 * c)); \
      if (cm) { _Pragma("unroll") for (int i = 0; i < 8; ++i) v[i] = __hip_atomic_load(((cm >> i) & 1u) ? pgp + i * 64 + lane : pgp, RLX_AGENT); } } while (0)
#define H1_CHECK(v, cm) do { if (cm) { _Pragma("unroll") for (int i = 0; i < 8; ++i) \
      if (((cm & need) >> i) & 1u) { if (__all((unsigned)(v[i] >> 32) == T32)) { stw[i * (STG_ROW / 4)] = (unsigned)v[i]; need &= ~(1u << i); } } } } while (0)
    _Pragma("unroll 1") for (unsigned sp = 0; sp < LSPIN; ++sp) {
      H1_ISSUE(va, ca);
      if (!pre && CT[C_WOK] != 0u) {
        CBAR();
        LAS unsigned char* wb = L + L_A + (8 * g + (r >> 2)) * 128 + c * 32 + (r & 3) * 8;
#pragma unroll
        for (int ks = 0; ks < 4; ++ks) { const v2u t0 = trread(wb + ks * 4096), t1 = trread(wb + ks * 4096 + 512); bw[ks] = v4u{t0.x, t0.y, t1.x, t1.y}; }
#pragma unroll
        for (int ks = 0; ks < 2; ++ks) {
          const v2u t0 = trread(wb + SLOT + ks * 4096), t1 = trread(wb + SLOT + ks * 4096 + 512);
          const v4u hf = *(const LAS v4u*)(L + L_HB + (4 * (r & 1) + (r >> 2)) * HB_ROW + 64 * ks + 16 * g);
          dff = mfma(hf, v4u{t0.x, t0.y, t1.x, t1.y}, dff);
        }
        pre = true;
        TS(5);
      }
      H1_CHECK(vb, cb);
      if (need == 0u && pre) break;
      __builtin_amdgcn_s_sleep(H1_SLEEP);
      H1_ISSUE(vb, cb);
      H1_CHECK(va, ca);
      if (need == 0u && pre) break;
      __builtin_amdgcn_s_sleep(H1_SLEEP);
    }
    TS(6);
    LDS_WAIT();
    CT[C_STG + c] = 1u;
#pragma unroll 1
    for (unsigned sp = 0; sp < LSPIN; ++sp) { const v4u fl = *(volatile LAS v4u*)(CT + C_STG); if ((fl.x & fl.y & fl.z & fl.w) && CT[C_SEEN] == ~0u) break; }
    CBAR();
    if (c == 3) TS(3);
    const LAS float* mls = (const LAS float*)(L + L_ML);
    LAS unsigned char* stg = L + L_RING + (r >> 3) * SLOT + 4096 + (r & 7) * STG_ROW + 16 * g;
#pragma unroll
    for (int rt = 0; rt < 2; ++rt) {
      f32x4 d = {0.f, 0.f, 0.f, 0.f};
#pragma unroll
      for (int ks = 0; ks < 4; ++ks) d = mfma(*(const LAS v4u*)(stg + 2 * rt * SLOT + 64 * ks), bw[ks], d);
      const f32x4 m01 = *(const LAS f32x4*)(mls + 32 * rt + 8 * g), m23 = *(const LAS f32x4*)(mls + 32 * rt + 8 * g + 4);
      const float Mx = fmaxf(fmaxf(m01[0], m01[2]), fmaxf(m23[0], m23[2]));
      const float w0 = __builtin_amdgcn_exp2f(m01[0] - Mx) * m01[1], w1 = __builtin_amdgcn_exp2f(m01[2] - Mx) * m01[3];
      const float w2 = __builtin_amdgcn_exp2f(m23[0] - Mx) * m23[1], w3 = __builtin_amdgcn_exp2f(m23[2] - Mx) * m23[3];
      const float gate = __uint_as_float(CT[C_G + 4 * rt + g]);
      const float att = ((w0 * d[0] + w1 * d[1]) + (w2 * d[2] + w3 * d[3])) / ((w0 + w1) + (w2 + w3));
      const float t = fmaf(gate, att, (1.f - gate) * dff[rt]);
      __hip_atomic_store(ws64 + W_OG / 8 + ((size_t)((4 * rt + g) * 32 + 4 * n8 + c) * 32 + ht) * 16 + r, ((u64)TAG_HI << 32) | (f24(t) | TAG_LO), RLX_AGENT);
    }
  }
  if (c == 0) TS(2);
  if (c == 0) {
    const unsigned oo = (unsigned)W_OG + (unsigned)(bid * 4096 + lane * 16);
    float t0 = 0.f, t1 = 0.f;
    v4u va[4], vb[4];
    bool ok;
#define H2_ISSUE(v) do { _Pragma("unroll") for (int i = 0; i < 4; ++i) v[i] = ld16(rs, oo + i * 1024u); } while (0)
#define H2_CHECK(v) do { t0 = 0.f; t1 = 0.f; ok = true; \
      _Pragma("unroll") for (int i = 0; i < 4; ++i) { ok &= TAG_OK(v[i].x, v[i].y) && TAG_OK(v[i].z, v[i].w); t0 += __uint_as_float(v[i].x << 8); t1 += __uint_as_float(v[i].z << 8); } } while (0)
    H2_ISSUE(va);
    _Pragma("unroll 1") for (unsigned sp = 0; sp < GSPIN; ++sp) {
      __builtin_amdgcn_s_sleep(H2_SLEEP);
      H2_ISSUE(vb);
      H2_CHECK(va);
      if (__all(ok)) break;
      __builtin_amdgcn_s_sleep(H2_SLEEP);
      H2_ISSUE(va);
      H2_CHECK(vb);
      if (__all(ok)) break;
    }
    t0 += dpp<0x128>(t0); t1 += dpp<0x128>(t1);
    t0 = xrow_sum(t0); t1 = xrow_sum(t1);
    if (lane < 8) ((GAS unsigned*)a.out)[bid * 8 + lane] = cvtpk(t0, t1);
    TS(3);
    if (bid == 0 && lane == 0) __hip_atomic_store(ws64 + W_EPOCH / 8, T64, RLX_AGENT);
  }
  TS_FLUSH(wave);
}
}

extern "C" void kernel_launch(void* const* d_in, const int* in_sizes, int n_in, void* d_out, int out_size, void* d_ws,
                              size_t ws_size, hipStream_t stream) {
  (void)in_sizes; (void)n_in; (void)out_size;
  if (ws_size < W_END) return;
  Args a;
  a.x = (const unsigned char*)d_in[0]; a.k = (const unsigned char*)d_in[1]; a.v = (const unsigned char*)d_in[2];
  a.wq = (const unsigned char*)d_in[3]; a.wo = (const unsigned char*)d_in[4];
  a.wg = (const unsigned char*)d_in[6]; a.wu = (const unsigned char*)d_in[7]; a.wd = (const unsigned char*)d_in[8];
  a.gw = (const unsigned char*)d_in[9];
  a.out = (unsigned char*)d_out; a.ws = (unsigned char*)d_ws;
  (void)hipFuncSetAttribute((const void*)fused_kernel, hipFuncAttributeMaxDynamicSharedMemorySize, L_BYTES);
  hipLaunchKernelGGL(fused_kernel, dim3(NWG), dim3(NTHR), L_BYTES, stream, a);
}
```

```cpp
#include <hip/hip_runtime.h>
#include <hip/hip_bf16.h>
#include <stdint.h>

#define GAS __attribute__((address_space(1)))
#define LAS __attribute__((address_space(3)))
typedef unsigned v4u __attribute__((ext_vector_type(4)));
typedef unsigned v2u __attribute__((ext_vector_type(2)));
typedef float f32x4 __attribute__((ext_vector_type(4)));
typedef float f32x2 __attribute__((ext_vector_type(2)));
typedef __bf16 bf16x8 __attribute__((ext_vector_type(8)));
typedef short s16x4 __attribute__((ext_vector_type(4)));
typedef unsigned long long u64;
typedef GAS u64 gu64;
#define RLX_AGENT __ATOMIC_RELAXED, __HIP_MEMORY_SCOPE_AGENT

namespace {
constexpr int NB = 8, DM = 512, HQ = 32, HKV = 8, SK = 8192, DH = 128, FF = 1792;
constexpr int NWG = 256, NTHR = 6 * 64;
constexpr int NTILE = 32;
#define SW 16
#define WO_AT 32
#define H1_DIRECT 16
#define TPAT 4
#define PACE_R 550
#define PACE_A 90
#define QG_AT 6u
#define H1_SLEEP 1
#define H2_SLEEP 1
#define ML_SLEEP 2
constexpr int SLOT = 16384;
constexpr int L_RING = 0, L_A = 8 * SLOT, L_B = L_A + SLOT, L_Q = L_B + 8192, L_HB = L_Q + 1024, L_ML = L_HB + 2048, L_CTRL = L_ML + 256, L_BYTES = L_CTRL + 256;
constexpr int STG_ROW = 272;
constexpr int HB_ROW = 144;
constexpr int C_LANDED = 0, C_GUFREE = 1, C_QRDY = 2, C_SEEN = 3, C_DONE = 4, C_MRG = 8, C_STG = 12, C_TAG = 16, C_TAGOK = 18, C_WOK = 19, C_CLEAN = 20, C_G = 24;
static_assert(L_BYTES <= 160 * 1024, "LDS");
constexpr size_t W_EPOCH = 0, W_G = 256, W_FLAG = 4096;
constexpr size_t W_QG = 8192;
constexpr size_t W_GP = W_QG + (size_t)2 * NB * 4096 * 8;
constexpr size_t W_HG = W_GP + (size_t)224 * 2048;
constexpr size_t W_PG = W_HG + (size_t)NB * FF * 8;
constexpr unsigned PGB = 16384 + 512;
constexpr size_t W_OG = W_PG + (size_t)HQ * PGB;
constexpr size_t W_END = W_OG + (size_t)NWG * 4096;
static_assert(W_END < (1u << 30) && W_PG % 1024 == 0, "buffer offsets are int");
constexpr u64 EPOCH_STEP = 0x9E3779B97F4A7C15ull;
constexpr unsigned GSPIN = 1u << 15, LSPIN = 1u << 18;
constexpr float SC2 = 0.08838834764831845f * 1.4426950408889634f;

struct Args {
  const unsigned char *x, *k, *v, *wq, *wo, *wg, *wu, *wd, *gw;
  unsigned char *out, *ws;
};

__device__ __forceinline__ f32x4 mfma(v4u a, v4u b, f32x4 c) {
  return __builtin_amdgcn_mfma_f32_16x16x32_bf16(__builtin_bit_cast(bf16x8, a), __builtin_bit_cast(bf16x8, b), c, 0, 0, 0);
}
__device__ __forceinline__ v2u trread(LAS unsigned char* p) {
  return __builtin_bit_cast(v2u, __builtin_amdgcn_ds_read_tr16_b64_v4i16((LAS s16x4*)p));
}
__device__ __forceinline__ unsigned cvtpk(float lo, float hi) {
  typedef __bf16 bf16x2 __attribute__((ext_vector_type(2)));
  bf16x2 r; r[0] = (__bf16)lo; r[1] = (__bf16)hi; return __builtin_bit_cast(unsigned, r);
}
template <int CTRL> __device__ __forceinline__ float dpp(float x) {
  return __builtin_bit_cast(float, __builtin_amdgcn_mov_dpp(__builtin_bit_cast(int, x), CTRL, 0xf, 0xf, true));
}
__device__ __forceinline__ float xrow_max(float x) {
  auto s = __builtin_amdgcn_permlane16_swap(__float_as_uint(x), __float_as_uint(x), false, false);
  x = fmaxf(__uint_as_float(s[0]), __uint_as_float(s[1]));
  auto t = __builtin_amdgcn_permlane32_swap(__float_as_uint(x), __float_as_uint(x), false, false);
  return fmaxf(__uint_as_float(t[0]), __uint_as_float(t[1]));
}
__device__ __forceinline__ float xrow_sum(float x) {
  auto s = __builtin_amdgcn_permlane16_swap(__float_as_uint(x), __float_as_uint(x), false, false);
  x = __uint_as_float(s[0]) + __uint_as_float(s[1]);
  auto t = __builtin_amdgcn_permlane32_swap(__float_as_uint(x), __float_as_uint(x), false, false);
  return __uint_as_float(t[0]) + __uint_as_float(t[1]);
}
__device__ __forceinline__ float bflo(unsigned u) { return __uint_as_float(u << 16); }
__device__ __forceinline__ float bfhi(unsigned u) { return __uint_as_float(u & 0xffff0000u); }

__device__ __forceinline__ v4u ld16(__amdgpu_buffer_rsrc_t rs, unsigned off) { return __builtin_amdgcn_raw_buffer_load_b128(rs, (int)off, 0, 16); }
__device__ __forceinline__ void st16(__amdgpu_buffer_rsrc_t rs, unsigned off, v4u v) { __builtin_amdgcn_raw_buffer_store_b128(v, rs, (int)off, 0, 16); }
__device__ __forceinline__ unsigned f24(float v) { return (__float_as_uint(v) + 0x80u) >> 8; }
#define TAG_LO ((unsigned)T64 << 24)
#define TAG_HI ((unsigned)(T64 >> 8))
#define TAG_OK(lo, hi) ((hi) == TAG_HI && ((lo) ^ TAG_LO) < 0x1000000u)
#define CBAR() asm volatile("" ::: "memory")
#define LDS_WAIT() asm volatile("s_waitcnt lgkmcnt(0)" ::: "memory")
#define KPOL " nt"
#define VPOL " nt"
#define WPOL " nt"
#define DMA4(sbase, dst, v0, v1, v2, v3, POL)                                                                    \
  asm volatile("s_mov_b32 m0, %5\n\ts_nop 0\n\t"                                                                 \
               "global_load_lds_dwordx4 %0, %4" POL "\n\t"                                                       \
               "global_load_lds_dwordx4 %1, %4 offset:1024" POL "\n\t"                                           \
               "global_load_lds_dwordx4 %2, %4 offset:2048" POL "\n\t"                                           \
               "global_load_lds_dwordx4 %3, %4 offset:3072" POL                                                  \
               :: "v"(v0), "v"(v1), "v"(v2), "v"(v3), "s"(sbase), "s"(dst) : "memory")
#define DMA1S(sbase, voff, dst)                                                                                  \
  asm volatile("s_mov_b32 m0, %2\n\ts_nop 0\n\tglobal_load_lds_dwordx4 %0, %1" WPOL :: "v"(voff), "s"(sbase), "s"(dst) : "memory")
#define DMA1V(vaddr, dst)                                                                                        \
  asm volatile("s_mov_b32 m0, %1\n\ts_nop 0\n\tglobal_load_lds_dwordx4 %0, off" WPOL :: "v"(vaddr), "s"(dst) : "memory")
#define VMWAIT(n) asm volatile("s_waitcnt vmcnt(" #n ")" ::: "memory")
#define VMWAITN(n) asm volatile("s_waitcnt vmcnt(%0)" :: "i"(n) : "memory")
#define TS(i) do { } while (0)
#define TS_FLUSH(w) do { } while (0)

__device__ __forceinline__ void lds_wait_ge(volatile LAS unsigned* w, unsigned need) {
#pragma unroll 1
  for (unsigned sp = 0; *w < need && sp < LSPIN; ++sp) __builtin_amdgcn_s_sleep(1);
  CBAR();
}

__global__ void __launch_bounds__(NTHR, 2) fused_kernel(Args a) {
  extern __shared__ __attribute__((aligned(16))) unsigned char smem[];
  LAS unsigned char* L = (LAS unsigned char*)smem;
  volatile LAS unsigned* CT = (volatile LAS unsigned*)(L + L_CTRL);
  const int tid = threadIdx.x, lane = tid & 63, wave = __builtin_amdgcn_readfirstlane(tid >> 6);
  const int bid = blockIdx.x;
  const int kap = bid >> 5, jg = bid & 31, b = jg >> 2, s = jg & 3;
  const int qcol = kap * 512 + (((jg & 7) << 1) | ((jg >> 3) & 1)) * 32, qk = jg >> 4;
  const bool has_ffn = (bid >> 3) < 28;
  const int fi = has_ffn ? (bid >> 3) : 27;
  const int fcg = (bid & 7) * 7 + (fi >> 2), fk = fi & 3;
  const int hh = jg >> 3, ht = kap * 4 + hh, n8 = jg & 7;
  const int g = lane >> 4, r = lane & 15;
  gu64* ws64 = (gu64*)a.ws;
  const __amdgpu_buffer_rsrc_t rs = __builtin_amdgcn_make_buffer_rsrc((void*)a.ws, 0, (int)W_END, 0x00020000);
  if (__builtin_expect(wave == 0, 1)) {
    __builtin_amdgcn_s_waitcnt(0);
    __builtin_amdgcn_s_setprio(3);
    const unsigned lb = (unsigned)(size_t)L;
    {
      const unsigned voff = (unsigned)(lane >> 2) * 8192u + (unsigned)(lane & 3) * 16u;
      const unsigned char* sb = a.wq + (size_t)qk * 256 * 8192 + (size_t)qcol * 2;
#pragma unroll 1
      for (int j = 0; j < 16; ++j) DMA1S(sb + (size_t)j * 16 * 8192, voff, lb + L_A + j * 1024);
    }
    {
      const unsigned char* p = ((lane & 32) ? a.wu : a.wg) + (size_t)(fk * 128 + ((lane >> 2) & 7)) * (FF * 2) + (size_t)fcg * 64 + (lane & 3) * 16;
#pragma unroll 1
      for (int j = 0; j < 16; ++j) DMA1V(p + (size_t)j * 8 * (FF * 2), lb + L_RING + 7 * SLOT + j * 1024);
    }
    __builtin_amdgcn_s_barrier();
    CBAR();
    unsigned ko[4], vo[2];
#pragma unroll
    for (int j = 0; j < 4; ++j) ko[j] = (unsigned)g * 256u + (unsigned)((r ^ g ^ (4 * j)) * 16);
#pragma unroll
    for (int j = 0; j < 2; ++j) vo[j] = (unsigned)g * 256u + (unsigned)((r ^ (2 * (4 * j + g))) * 16);
    const size_t seq_off = (size_t)(b * HKV + kap) * SK * 256;
    const unsigned char* kb = a.k + seq_off;
    const unsigned char* vb = a.v + seq_off;
#define LOAD_WO(last) do { \
      const unsigned voff = (unsigned)(lane >> 3) * 1024u + (unsigned)(lane & 7) * 16u; \
      const unsigned char* sb = a.wo + (size_t)ht * 128 * 1024 + (size_t)n8 * 128; \
      const unsigned char* sd = a.wd + (size_t)ht * 56 * 1024 + (size_t)n8 * 128; \
      _Pragma("unroll 1") for (int j = 0; j < 23; ++j) { \
        DMA1S(j < 16 ? sb + (size_t)j * 8192 : sd + (size_t)(j - 16) * 8192, voff, lb + L_A + j * 1024); \
        if (last) { if (j == 7) { VMWAIT(8); CT[C_LANDED] = 2u + 2u * NTILE; } } \
        else if ((j & 7) == 7) VMWAIT(16); \
      } } while (0)
    unsigned seen = 0;
    const unsigned pace0 = (unsigned)__builtin_amdgcn_s_memrealtime();
    bool deep = true;
#pragma unroll 1
    for (int n = 0; n < NTILE; ++n) {
      const int tile = ((n + 4 * b) & 31) * 4 + s;
      const int cs = n & 3;
      if (n == WO_AT) LOAD_WO(false);
      if (n == WO_AT + 2) CT[C_WOK] = 1u;
#pragma unroll
      for (int hv = 0; hv < 2; ++hv) {
        const unsigned f = 2u + 2u * n + hv;
        if (n >= 4 || (n == 3 && hv == 1)) {
          volatile LAS unsigned* fw = n >= 4 ? (CT + C_DONE + cs) : (CT + C_GUFREE);
          const unsigned need = n >= 4 ? (unsigned)(2 * (n >> 2) - 1 + hv) : 1u;
          if (n < 4 || seen < need) {
            if (*fw < need) {
              VMWAIT(0); CT[C_LANDED] = f;
              lds_wait_ge(fw, need);
            }
          }
        }
        const unsigned char* sb = (hv ? vb : kb) + (size_t)tile * SLOT;
        const unsigned dst = lb + L_RING + (hv * 4 + cs) * SLOT;
        if (hv == 0) { DMA4(sb, dst, ko[0], ko[1], ko[2], ko[3], KPOL); DMA4(sb + 4096, dst + 4096, ko[0], ko[1], ko[2], ko[3], KPOL); }
        else         { DMA4(sb, dst, vo[0], vo[1], vo[0], vo[1], VPOL); DMA4(sb + 4096, dst + 4096, vo[0], vo[1], vo[0], vo[1], VPOL); }
        if (deep) VMWAIT(32); else VMWAIT(16);
        if (hv == 0) { DMA4(sb + 8192, dst + 8192, ko[0], ko[1], ko[2], ko[3], KPOL); DMA4(sb + 12288, dst + 12288, ko[0], ko[1], ko[2], ko[3], KPOL); }
        else         { DMA4(sb + 8192, dst + 8192, vo[0], vo[1], vo[0], vo[1], VPOL); DMA4(sb + 12288, dst + 12288, vo[0], vo[1], vo[0], vo[1], VPOL); }
        const unsigned nx = CT[C_DONE + (hv ? ((cs + 1) & 3) : cs)];
        const unsigned now = (unsigned)__builtin_amdgcn_s_memrealtime() - pace0;
        if (deep) { VMWAIT(32); CT[C_LANDED] = f - 1; } else { VMWAIT(16); CT[C_LANDED] = f; }
        deep = now > (unsigned)(PACE_A + PACE_R * (int)f / 10 + (n >= WO_AT ? 80 : 0)) && f < 2u * NTILE;
        seen = __builtin_amdgcn_readfirstlane(nx);
        if (f == 12) TS(1);
        if (f == 3) TS(5);
        if (f == 6) TS(6);
        if (f == 34) TS(7);
      }
    }
    {
      if (WO_AT < NTILE) {
        VMWAIT(16); CT[C_LANDED] = 1u + 2u * NTILE;
        VMWAIT(0); CT[C_LANDED] = 2u + 2u * NTILE;
        TS(2);
      } else {
        VMWAIT(16); CT[C_LANDED] = 1u + 2u * NTILE;
        LOAD_WO(true);
        TS(2);
        VMWAIT(0);
      }
      CT[C_WOK] = 1u;
      TS(3);
      TS_FLUSH(0);
    }
    return;
  }

  const int c = wave - 1;
  u64 T64 = 0;
  v4u xf[8];
  if (wave == 5) T64 = __hip_atomic_load(ws64 + W_EPOCH / 8, RLX_AGENT);
  if (wave <= 2) {
    const GAS v4u* xp = (const GAS v4u*)(a.x + (size_t)(r & 7) * 1024 + (wave == 1 ? qk * 512 : fk * 256) + g * 16);
#pragma unroll
    for (int ks = 0; ks < 8; ++ks) xf[ks] = xp[(wave == 1 ? ks : (ks & 3)) * 4];
  }
  if (wave == 3) ((LAS unsigned*)(L + L_CTRL))[lane] = 0u;
  if (wave == 4) ((LAS v4u*)(L + L_B + 7168))[lane] = v4u{0u, 0u, 0u, 0u};
  LDS_WAIT();
  __builtin_amdgcn_s_barrier();
  CBAR();

  if (wave == 5) {
    T64 += EPOCH_STEP;
    if (lane == 0) { CT[C_TAG] = (unsigned)T64; CT[C_TAG + 1] = (unsigned)(T64 >> 32); LDS_WAIT(); CT[C_TAGOK] = 1u; }
    if (bid == NWG - 1) {
      const int gb = lane >> 3, part = lane & 7;
      const GAS v4u* xp = (const GAS v4u*)(a.x + (size_t)gb * 1024 + part * 128);
      const GAS v4u* gp = (const GAS v4u*)(a.gw + part * 128);
      float ga = 0.f;
#pragma unroll
      for (int i = 0; i < 8; ++i) {
        const v4u xv = xp[i], gv = gp[i];
#pragma unroll
        for (int e = 0; e < 4; ++e) { ga = fmaf(bflo(xv[e]), bflo(gv[e]), ga); ga = fmaf(bfhi(xv[e]), bfhi(gv[e]), ga); }
      }
      ga += dpp<0xB1>(ga); ga += dpp<0x4E>(ga); ga += dpp<0x141>(ga);
      const float gate = 1.f / (1.f + __expf(-ga));
      if (part == 0) __hip_atomic_store(ws64 + W_G / 8 + gb, ((u64)TAG_HI << 32) | (f24(gate) | TAG_LO), RLX_AGENT);
    }
    {
      lds_wait_ge(CT + C_LANDED, QG_AT);
      const unsigned qo = (unsigned)W_QG + (unsigned)((b * HKV + kap) * 8192 + lane * 16);
      _Pragma("unroll 1") for (unsigned sp = 0; sp < GSPIN; ++sp) {
        v4u v[8]; bool ok = true;
#pragma unroll
        for (int i = 0; i < 8; ++i) v[i] = ld16(rs, qo + i * 1024u);
#pragma unroll
        for (int i = 0; i < 4; ++i) {
          ok &= TAG_OK(v[i].x, v[i].y) && TAG_OK(v[i].z, v[i].w) && TAG_OK(v[i + 4].x, v[i + 4].y) && TAG_OK(v[i + 4].z, v[i + 4].w);
          ((LAS unsigned*)(L + L_Q))[i * 64 + lane] = cvtpk(__uint_as_float(v[i].x << 8) + __uint_as_float(v[i + 4].x << 8), __uint_as_float(v[i].z << 8) + __uint_as_float(v[i + 4].z << 8));
        }
        if (__all(ok)) break;
        __builtin_amdgcn_s_sleep(8);
      }
      LDS_WAIT(); CT[C_QRDY] = 1u;
      TS(2);
    }
    {
      lds_wait_ge(CT + C_LANDED, 12u);
      const int fb = lane < 32 ? kap * 32 + lane : (bid & 7) + 8 * ((fi & ~3) + (lane & 3));
      _Pragma("unroll 1") for (unsigned sp = 0; sp < GSPIN; ++sp) {
        if (__all(__hip_atomic_load(ws64 + W_FLAG / 8 + fb, RLX_AGENT) == T64)) break;
        __builtin_amdgcn_s_sleep(32);
      }
      CBAR();
    }
    if (has_ffn) {
      const unsigned go = (unsigned)W_GP + (unsigned)((fcg * 4 + fk) * 2048 + lane * 16);
      const f32x4 p0 = __builtin_bit_cast(f32x4, ld16(rs, go)), p1 = __builtin_bit_cast(f32x4, ld16(rs, go + 1024u));
      unsigned hw[4];
#pragma unroll
      for (int e = 0; e < 4; ++e) {
        const unsigned t = __float_as_uint(p0[e] + p1[e]);
        auto s32 = __builtin_amdgcn_permlane32_swap(t, t, false, false);
        const unsigned tot = __float_as_uint(__uint_as_float(s32[0]) + __uint_as_float(s32[1]));
        auto s16 = __builtin_amdgcn_permlane16_swap(tot, tot, false, false);
        const float gt = __uint_as_float(s16[0]), up = __uint_as_float(s16[1]);
        const float u = 0.7978845608028654f * (gt + 0.044715f * gt * gt * gt);
        const float th = 1.f - 2.f / (__expf(2.f * u) + 1.f);
        hw[e] = f24(0.5f * gt * (1.f + th) * up) | TAG_LO;
      }
      const int src = ((2 * (g & 1) + (r >> 3)) * 4 + ((r & 7) >> 1)) * 4;
      const unsigned a0 = __builtin_amdgcn_ds_bpermute(src, (int)hw[0]), a1 = __builtin_amdgcn_ds_bpermute(src, (int)hw[1]);
      const unsigned a2 = __builtin_amdgcn_ds_bpermute(src, (int)hw[2]), a3 = __builtin_amdgcn_ds_bpermute(src, (int)hw[3]);
      if (lane < 32)
        st16(rs, (unsigned)W_HG + (unsigned)((4 * (fk & 1) + 2 * g + (r >> 3)) * FF + fcg * 32 + (fk >> 1) * 16 + 2 * (r & 7)) * 8u,
             v4u{(r & 1) ? a2 : a0, TAG_HI, (r & 1) ? a3 : a1, TAG_HI});
    }
    {
      lds_wait_ge(CT + C_LANDED, 20u);
      const bool pad = (lane & 31) >= 28;
      const unsigned ho = (unsigned)W_HG + (unsigned)((lane >> 5) * FF + ht * 56 + (pad ? 0 : 2 * (lane & 31))) * 8u;
      _Pragma("unroll 1") for (unsigned sp = 0; sp < GSPIN; ++sp) {
        v4u v[4];
#pragma unroll
        for (int i = 0; i < 4; ++i) v[i] = ld16(rs, ho + (unsigned)(i * 2 * FF * 8));
        const u64 gv = __hip_atomic_load(ws64 + W_G / 8 + (lane & 7), RLX_AGENT);
        bool ok = TAG_OK((unsigned)gv, (unsigned)(gv >> 32));
#pragma unroll
        for (int i = 0; i < 4; ++i) {
          ok &= TAG_OK(v[i].x, v[i].y) && TAG_OK(v[i].z, v[i].w);
          *(LAS unsigned*)(L + L_HB + (2 * i + (lane >> 5)) * HB_ROW + (lane & 31) * 4) =
              pad ? 0u : cvtpk(__uint_as_float(v[i].x << 8), __uint_as_float(v[i].z << 8));
        }
        if (lane < 8) CT[C_G + lane] = (unsigned)gv << 8;
        if (__all(ok)) break;
        __builtin_amdgcn_s_sleep(32);
      }
      LDS_WAIT();
      CT[C_CLEAN] = 1u;
      TS(1);
    }
    {
      lds_wait_ge(CT + C_LANDED, 2u * NTILE - 2u);
      gu64* mlp = (gu64*)(a.ws + W_PG + (size_t)ht * PGB + 16384) + lane;
      u64 ma = __hip_atomic_load(mlp, RLX_AGENT), mb;
#define ML_CHECK(v) do { const bool mk = TAG_OK((unsigned)v, (unsigned)(v >> 32)); if (mk) ((LAS unsigned*)(L + L_ML))[lane] = (unsigned)v << 8; \
        u64 k = __ballot(mk); k &= k >> 1; k &= 0x5555555555555555ull; \
        k = (k | (k >> 1)) & 0x3333333333333333ull; k = (k | (k >> 2)) & 0x0f0f0f0f0f0f0f0full; k = (k | (k >> 4)) & 0x00ff00ff00ff00ffull; \
        k = (k | (k >> 8)) & 0x0000ffff0000ffffull; k = (k | (k >> 16)) & 0xffffffffull; \
        sn = (unsigned)k; LDS_WAIT(); CT[C_SEEN] = sn; } while (0)
      unsigned sn;
      _Pragma("unroll 1") for (unsigned sp = 0; sp < GSPIN; ++sp) {
        __builtin_amdgcn_s_sleep(ML_SLEEP);
        mb = __hip_atomic_load(mlp, RLX_AGENT);
        ML_CHECK(ma);
        if (sn == ~0u) break;
        __builtin_amdgcn_s_sleep(ML_SLEEP);
        ma = __hip_atomic_load(mlp, RLX_AGENT);
        ML_CHECK(mb);
        if (sn == ~0u) break;
      }
    }
    TS(3);
    {
      const unsigned oo = (unsigned)W_OG + (unsigned)(bid * 4096), ol = (unsigned)lane * 16u;
      float t0[4], t1[4];
      v4u va[4], vb[4];
      unsigned need = 15u, ca, cb;
#define H2_ISSUE(v, cm) do { cm = need; _Pragma("unroll") for (int i = 0; i < 4; ++i) v[i] = ld16(rs, ((cm >> i) & 1u) ? oo + i * 1024u + ol : oo); } while (0)
#define H2_CHECK(v, cm) do { _Pragma("unroll") for (int i = 0; i < 4; ++i) if (((cm & need) >> i) & 1u) { \
        if (__all(TAG_OK(v[i].x, v[i].y) && TAG_OK(v[i].z, v[i].w))) { t0[i] = __uint_as_float(v[i].x << 8); t1[i] = __uint_as_float(v[i].z << 8); need &= ~(1u << i); } } } while (0)
#pragma unroll 1
      for (unsigned sp = 0; sp < LSPIN; ++sp) { const v4u fl = *(volatile LAS v4u*)(CT + C_STG); if (fl.x & fl.y & fl.z & fl.w) break; __builtin_amdgcn_s_sleep(1); }
      H2_ISSUE(va, ca);
      _Pragma("unroll 1") for (unsigned sp = 0; sp < GSPIN; ++sp) {
        __builtin_amdgcn_s_sleep(H2_SLEEP);
        H2_ISSUE(vb, cb);
        H2_CHECK(va, ca);
        if (need == 0u) break;
        __builtin_amdgcn_s_sleep(H2_SLEEP);
        H2_ISSUE(va, ca);
        H2_CHECK(vb, cb);
        if (need == 0u) break;
      }
      float s0 = (t0[0] + t0[1]) + (t0[2] + t0[3]), s1 = (t1[0] + t1[1]) + (t1[2] + t1[3]);
      s0 += dpp<0x128>(s0); s1 += dpp<0x128>(s1);
      s0 = xrow_sum(s0); s1 = xrow_sum(s1);
      if (lane < 8) ((GAS unsigned*)a.out)[bid * 8 + lane] = cvtpk(s0, s1);
      TS(4);
      if (bid == 0 && lane == 0) __hip_atomic_store(ws64 + W_EPOCH / 8, T64, RLX_AGENT);
    }
    TS_FLUSH(5);
    return;
  }

  if (c == 0) {
    lds_wait_ge(CT + C_TAGOK, 1u);
    T64 = ((u64)CT[C_TAG + 1] << 32) | CT[C_TAG];
    lds_wait_ge(CT + C_LANDED, 1u);
    TS(0);
    LAS unsigned char* sl = L + L_A + (8 * g + (r >> 2)) * 64 + (r & 3) * 8;
    f32x4 d[2] = {{0.f, 0.f, 0.f, 0.f}, {0.f, 0.f, 0.f, 0.f}};
#pragma unroll
    for (int ks = 0; ks < 8; ++ks)
#pragma unroll
      for (int ct = 0; ct < 2; ++ct) {
        const v2u b0 = trread(sl + ks * 2048 + ct * 32), b1 = trread(sl + ks * 2048 + ct * 32 + 256);
        d[ct] = mfma(xf[ks], v4u{b0.x, b0.y, b1.x, b1.y}, d[ct]);
      }
    const f32x4 dv = g < 2 ? d[0] : d[1];
    gu64* qg = ws64 + W_QG / 8 + (size_t)((4 * (g & 1) * HKV + kap) * 2 + qk) * 512 + (qcol & 511) + (g >> 1) * 16 + r;
#pragma unroll
    for (int e = 0; e < 4; ++e) __hip_atomic_store(qg + (size_t)e * HKV * 1024, ((u64)TAG_HI << 32) | (f24(dv[e]) | TAG_LO), RLX_AGENT);
    TS(1);
  } else if (c == 1) {
    lds_wait_ge(CT + C_TAGOK, 1u);
    T64 = ((u64)CT[C_TAG + 1] << 32) | CT[C_TAG];
    const v4u inv = {0u, ~(unsigned)T64, 0u, ~(unsigned)T64};
    const unsigned co = (unsigned)W_PG + (unsigned)(kap * 4 + (lane >> 5)) * PGB + (unsigned)(jg * 512 + (lane & 31) * 16);
    st16(rs, co, inv);
    st16(rs, co + 2u * PGB, inv);
    lds_wait_ge(CT + C_LANDED, 2u);
    LAS unsigned char* sl = L + L_RING + 7 * SLOT + g * 1024 + (r >> 2) * 64 + (r & 3) * 8;
    f32x4 d[4] = {{0.f, 0.f, 0.f, 0.f}, {0.f, 0.f, 0.f, 0.f}, {0.f, 0.f, 0.f, 0.f}, {0.f, 0.f, 0.f, 0.f}};
#pragma unroll
    for (int ks = 0; ks < 4; ++ks)
#pragma unroll
      for (int mc = 0; mc < 4; ++mc) {
        LAS unsigned char* q = sl + ks * 4096 + (mc >> 1) * 512 + (mc & 1) * 32;
        const v2u b0 = trread(q), b1 = trread(q + 256);
        d[mc] = mfma(xf[ks], v4u{b0.x, b0.y, b1.x, b1.y}, d[mc]);
      }
    LDS_WAIT();
    CT[C_GUFREE] = 1u;
    if (has_ffn) {
      const f32x4 dg = g < 2 ? d[0] : d[1], du = g < 2 ? d[2] : d[3];
      GAS unsigned* gp = (GAS unsigned*)(a.ws + W_GP + (size_t)(fcg * 4 + 2 * (g >> 1) + (g & 1)) * 2048 + fk * 512) + r;
#pragma unroll
      for (int e = 0; e < 4; ++e) {
        __hip_atomic_store(gp + e * 16, __float_as_uint(dg[e]), RLX_AGENT);
        __hip_atomic_store(gp + 64 + e * 16, __float_as_uint(du[e]), RLX_AGENT);
      }
    }
    VMWAIT(0);
    if (lane == 0) __hip_atomic_store(ws64 + W_FLAG / 8 + bid, T64, RLX_AGENT);
  } else {
    lds_wait_ge(CT + C_TAGOK, 1u);
    T64 = ((u64)CT[C_TAG + 1] << 32) | CT[C_TAG];
  }
  const unsigned T32 = (unsigned)T64;
  const v4u ones = {0x3F803F80u, 0x3F803F80u, 0x3F803F80u, 0x3F803F80u};
  LAS unsigned char* kbase = L + L_RING + c * SLOT + r * 256;
  LAS unsigned char* vbase = L + L_RING + (4 + c) * SLOT + (4 * g + (r >> 2)) * 256 + (r & 3) * 8;
  const int vx = 4 * (g & 1) + (r >> 2);

  v4u qf[4];
  f32x4 acc[8], accl = {0.f, 0.f, 0.f, 0.f};
#pragma unroll
  for (int m = 0; m < 8; ++m) acc[m] = f32x4{0.f, 0.f, 0.f, 0.f};
  float mrun = -INFINITY, fown = 0.f;
  f32x4 basea = {0.f, 0.f, 0.f, 0.f}, baseb = {0.f, 0.f, 0.f, 0.f};
#pragma unroll 1
  for (int lap = 0; lap < NTILE / 4; ++lap) {
    const unsigned n = 4u * lap + c;
    lds_wait_ge(CT + C_LANDED, 3u + 2u * n);
    v4u kf[4][4];
#pragma unroll
    for (int T = 0; T < 4; ++T)
#pragma unroll
      for (int ks = 0; ks < 4; ++ks) kf[T][ks] = *(const LAS v4u*)(kbase + T * 4096 + (((4 * ks + g) ^ r) * 16));
    LDS_WAIT();
    CT[C_DONE + c] = (unsigned)(2 * lap + 1);
#pragma unroll
    for (int T = 0; T < 4; ++T) asm volatile("" : "+v"(kf[T][0]), "+v"(kf[T][1]), "+v"(kf[T][2]), "+v"(kf[T][3]) :: "memory");
    v4u vf[2][8];
#define READ_V() do { \
      lds_wait_ge(CT + C_LANDED, 4u + 2u * n); \
      _Pragma("unroll") for (int ks = 0; ks < 2; ++ks) \
        _Pragma("unroll") for (int m = 0; m < 8; ++m) { \
          LAS unsigned char* p = vbase + ks * 8192 + ((m ^ vx) * 32); \
          const v2u t0 = trread(p), t1 = trread(p + 4096); \
          vf[ks][m] = v4u{t0.x, t0.y, t1.x, t1.y}; \
        } \
      LDS_WAIT(); \
      CT[C_DONE + c] = (unsigned)(2 * lap + 2); \
      _Pragma("unroll") for (int ks = 0; ks < 2; ++ks) { \
        asm volatile("" : "+v"(vf[ks][0]), "+v"(vf[ks][1]), "+v"(vf[ks][2]), "+v"(vf[ks][3]) :: "memory"); \
        asm volatile("" : "+v"(vf[ks][4]), "+v"(vf[ks][5]), "+v"(vf[ks][6]), "+v"(vf[ks][7]) :: "memory"); \
      } } while (0)
    if (lap == 0) {
      READ_V();
      lds_wait_ge(CT + C_QRDY, 1u);
#pragma unroll
      for (int ks = 0; ks < 4; ++ks) qf[ks] = *(const LAS v4u*)(L + L_Q + ((r >> 2) * 128 + 32 * ks + 8 * g) * 2);
    }
    f32x4 sc[4];
#pragma unroll
    for (int T = 0; T < 4; ++T) {
      f32x4 d = {0.f, 0.f, 0.f, 0.f};
#pragma unroll
      for (int ks = 0; ks < 4; ++ks) d = mfma(kf[T][ks], qf[ks], d);
      sc[T] = d;
    }
    float tm = fmaxf(fmaxf(sc[0][0], sc[0][1]), fmaxf(sc[0][2], sc[0][3]));
#pragma unroll
    for (int T = 1; T < 4; ++T) tm = fmaxf(tm, fmaxf(fmaxf(sc[T][0], sc[T][1]), fmaxf(sc[T][2], sc[T][3])));
    tm = xrow_max(tm);
    if (__any(tm > mrun)) {
      const float mn = fmaxf(mrun, tm);
      const float al = __builtin_amdgcn_exp2f((mrun - mn) * SC2);
#pragma unroll
      for (int m = 0; m < 8; ++m) acc[m] *= al;
      accl *= al;
      mrun = mn;
    }
    const float mo = -mrun * SC2;
    v4u pf[2];
#pragma unroll
    for (int T = 0; T < 4; ++T) {
      float p[4];
#pragma unroll
      for (int e = 0; e < 4; ++e) p[e] = __builtin_amdgcn_exp2f(fmaf(sc[T][e], SC2, mo));
      pf[T >> 1][(T & 1) * 2] = cvtpk(p[0], p[1]);
      pf[T >> 1][(T & 1) * 2 + 1] = cvtpk(p[2], p[3]);
    }
    accl = mfma(ones, pf[0], accl);
    accl = mfma(ones, pf[1], accl);
    asm volatile("" : "+v"(pf[0]), "+v"(pf[1]), "+v"(accl) :: "memory");
    if (n == NTILE - 1) {
      float mw[3], lw[3]; f32x4 oa[3], ob[3];
#pragma unroll
      for (int w = 0; w < 3; ++w) {
        lds_wait_ge(CT + C_MRG + w, 1u);
        const LAS float* rw = (const LAS float*)(L + L_RING + w * SLOT);
        mw[w] = rw[512 + (r >> 2)]; lw[w] = rw[516 + (r >> 2)];
        oa[w] = *(const LAS f32x4*)(rw + r * 32 + 4 * g); ob[w] = *(const LAS f32x4*)(rw + r * 32 + 16 + 4 * g);
      }
      const unsigned m24 = f24(fmaxf(fmaxf(mrun, mw[0]), fmaxf(mw[1], mw[2])) * SC2);
      const float Mr = __uint_as_float(m24 << 8);
      fown = __builtin_amdgcn_exp2f(fmaf(mrun, SC2, -Mr));
      float Ls = fown * accl[0];
#pragma unroll
      for (int w = 0; w < 3; ++w) {
        const float f = __builtin_amdgcn_exp2f(fmaf(mw[w], SC2, -Mr));
        Ls = fmaf(f, lw[w], Ls); basea += f * oa[w]; baseb += f * ob[w];
      }
      const float inv = 1.f / Ls;
      fown *= inv; basea *= inv; baseb *= inv;
      if ((lane & 51) == 0) st16(rs, (unsigned)W_PG + (unsigned)(kap * 4 + (r >> 2)) * PGB + (unsigned)(16384 + jg * 16), v4u{m24 | TAG_LO, TAG_HI, f24(Ls) | TAG_LO, TAG_HI});
    }
    if (lap) READ_V();
#pragma unroll
    for (int ks = 0; ks < 2; ++ks)
#pragma unroll
      for (int m = 0; m < 8; ++m) acc[m] = mfma(vf[ks][m], pf[ks], acc[m]);
  }
  TS(4);
  if (c == 3) {
    const int u = r & 3;
    const f32x4 sa = u == 0 ? acc[0] : u == 1 ? acc[2] : u == 2 ? acc[4] : acc[6], sb = u == 0 ? acc[1] : u == 1 ? acc[3] : u == 2 ? acc[5] : acc[7];
    const f32x4 xa = fown * sa + basea, xb = fown * sb + baseb;
    const unsigned po = (unsigned)W_PG + (unsigned)(kap * 4 + (r >> 2)) * PGB + (unsigned)(jg * 512 + g * 64 + u * 16);
    st16(rs, po, v4u{cvtpk(xa[0], xa[1]), T32, cvtpk(xa[2], xa[3]), T32});
    st16(rs, po + 256u, v4u{cvtpk(xb[0], xb[1]), T32, cvtpk(xb[2], xb[3]), T32});
    TS(2);
  } else {
    LAS float* rec = (LAS float*)(L + L_RING + c * SLOT);
    if ((r & 3) == 0) {
#pragma unroll
      for (int m = 0; m < 8; ++m) *(LAS f32x4*)(rec + (r >> 2) * 128 + 16 * m + 4 * g) = acc[m];
      if (g == 0) { rec[512 + (r >> 2)] = mrun; rec[516 + (r >> 2)] = accl[0]; }
    }
    LDS_WAIT();
    CT[C_MRG + c] = 1u;
  }
  lds_wait_ge(CT + C_CLEAN, 1u);
  {
    LAS unsigned* stw = (LAS unsigned*)(L + L_RING + c * SLOT + 4096) + 16 * ((lane >> 1) & 3) + 8 * (lane >> 5) + 2 * ((lane >> 3) & 3) + (lane & 1);
    gu64* pgp = (gu64*)(a.ws + W_PG + (size_t)ht * PGB + c * 4096);
    unsigned need = 0xffu, ca = 0u, cb = 0u;
    u64 va[8], vb[8];
#define H1_ISSUE(v, cm) do { const unsigned sn_ = CT[C_SEEN]; cm = __builtin_popcount(sn_) >= 32 - H1_DIRECT ? need : need & (sn_ >> (8 * c)); \
      if (cm) { _Pragma("unroll") for (int i = 0; i < 8; ++i) v[i] = __hip_atomic_load(((cm >> i) & 1u) ? pgp + i * 64 + lane : pgp, RLX_AGENT); } } while (0)
#define H1_CHECK(v, cm) do { if (cm) { _Pragma("unroll") for (int i = 0; i < 8; ++i) \
      if (((cm & need) >> i) & 1u) { if (__all((unsigned)(v[i] >> 32) == T32)) { stw[i * (STG_ROW / 4)] = (unsigned)v[i]; need &= ~(1u << i); } } } } while (0)
    _Pragma("unroll 1") for (unsigned sp = 0; sp < LSPIN; ++sp) {
      H1_ISSUE(va, ca);
      H1_CHECK(vb, cb);
      if (need == 0u) break;
      __builtin_amdgcn_s_sleep(H1_SLEEP);
      H1_ISSUE(vb, cb);
      H1_CHECK(va, ca);
      if (need == 0u) break;
      __builtin_amdgcn_s_sleep(H1_SLEEP);
    }
    TS(6);
    LDS_WAIT();
    CT[C_STG + c] = 1u;
    lds_wait_ge(CT + C_WOK, 1u);
    v4u bw[4]; f32x4 dff = {0.f, 0.f, 0.f, 0.f};
    {
      LAS unsigned char* wb = L + L_A + (8 * g + (r >> 2)) * 128 + c * 32 + (r & 3) * 8;
#pragma unroll
      for (int ks = 0; ks < 4; ++ks) { const v2u t0 = trread(wb + ks * 4096), t1 = trread(wb + ks * 4096 + 512); bw[ks] = v4u{t0.x, t0.y, t1.x, t1.y}; }
#pragma unroll
      for (int ks = 0; ks < 2; ++ks) {
        const v2u t0 = trread(wb + SLOT + ks * 4096), t1 = trread(wb + SLOT + ks * 4096 + 512);
        const v4u hf = *(const LAS v4u*)(L + L_HB + (4 * (r & 1) + (r >> 2)) * HB_ROW + 64 * ks + 16 * g);
        dff = mfma(hf, v4u{t0.x, t0.y, t1.x, t1.y}, dff);
      }
    }
    asm volatile("" : "+v"(bw[0]), "+v"(bw[1]), "+v"(bw[2]), "+v"(bw[3]), "+v"(dff) :: "memory");
    TS(5);
#pragma unroll 1
    for (unsigned sp = 0; sp < LSPIN; ++sp) { const v4u fl = *(volatile LAS v4u*)(CT + C_STG); if ((fl.x & fl.y & fl.z & fl.w) && CT[C_SEEN] == ~0u) break; }
    CBAR();
    if (c == 3) TS(3);
    const LAS float* mls = (const LAS float*)(L + L_ML);
    LAS unsigned char* stg = L + L_RING + (r >> 3) * SLOT + 4096 + (r & 7) * STG_ROW + 16 * g;
#pragma unroll
    for (int rt = 0; rt < 2; ++rt) {
      f32x4 d = {0.f, 0.f, 0.f, 0.f};
#pragma unroll
      for (int ks = 0; ks < 4; ++ks) d = mfma(*(const LAS v4u*)(stg + 2 * rt * SLOT + 64 * ks), bw[ks], d);
      const f32x4 m01 = *(const LAS f32x4*)(mls + 32 * rt + 8 * g), m23 = *(const LAS f32x4*)(mls + 32 * rt + 8 * g + 4);
      const float Mx = fmaxf(fmaxf(m01[0], m01[2]), fmaxf(m23[0], m23[2]));
      const float w0 = __builtin_amdgcn_exp2f(m01[0] - Mx) * m01[1], w1 = __builtin_amdgcn_exp2f(m01[2] - Mx) * m01[3];
      const float w2 = __builtin_amdgcn_exp2f(m23[0] - Mx) * m23[1], w3 = __builtin_amdgcn_exp2f(m23[2] - Mx) * m23[3];
      const float gate = __uint_as_float(CT[C_G + 4 * rt + g]);
      const float att = ((w0 * d[0] + w1 * d[1]) + (w2 * d[2] + w3 * d[3])) / ((w0 + w1) + (w2 + w3));
      const float t = fmaf(gate, att, (1.f - gate) * dff[rt]);
      __hip_atomic_store(ws64 + W_OG / 8 + ((size_t)((4 * rt + g) * 32 + 4 * n8 + c) * 32 + ht) * 16 + r, ((u64)TAG_HI << 32) | (f24(t) | TAG_LO), RLX_AGENT);
    }
  }
  if (c == 0) TS(2);
  TS_FLUSH(wave);
}
}

extern "C" void kernel_launch(void* const* d_in, const int* in_sizes, int n_in, void* d_out, int out_size, void* d_ws,
                              size_t ws_size, hipStream_t stream) {
  (void)in_sizes; (void)n_in; (void)out_size;
  if (ws_size < W_END) return;
  Args a;
  a.x = (const unsigned char*)d_in[0]; a.k = (const unsigned char*)d_in[1]; a.v = (const unsigned char*)d_in[2];
  a.wq = (const unsigned char*)d_in[3]; a.wo = (const unsigned char*)d_in[4];
  a.wg = (const unsigned char*)d_in[6]; a.wu = (const unsigned char*)d_in[7]; a.wd = (const unsigned char*)d_in[8];
  a.gw = (const unsigned char*)d_in[9];
  a.out = (unsigned char*)d_out; a.ws = (unsigned char*)d_ws;
  (void)hipFuncSetAttribute((const void*)fused_kernel, hipFuncAttributeMaxDynamicSharedMemorySize, L_BYTES);
  hipLaunchKernelGGL(fused_kernel, dim3(NWG), dim3(NTHR), L_BYTES, stream, a);
}
```

```cpp
#include <hip/hip_runtime.h>
#include <hip/hip_bf16.h>
#include <stdint.h>

#define GAS __attribute__((address_space(1)))
#define LAS __attribute__((address_space(3)))
typedef unsigned v4u __attribute__((ext_vector_type(4)));
typedef unsigned v2u __attribute__((ext_vector_type(2)));
typedef float f32x4 __attribute__((ext_vector_type(4)));
typedef float f32x2 __attribute__((ext_vector_type(2)));
typedef __bf16 bf16x8 __attribute__((ext_vector_type(8)));
typedef short s16x4 __attribute__((ext_vector_type(4)));
typedef unsigned long long u64;
typedef GAS u64 gu64;
#define RLX_AGENT __ATOMIC_RELAXED, __HIP_MEMORY_SCOPE_AGENT

namespace {
constexpr int NB = 8, DM = 512, HQ = 32, HKV = 8, SK = 8192, DH = 128, FF = 1792;
constexpr int NWG = 256, NTHR = 6 * 64;
constexpr int NTILE = 32;
#define SW 16
#define WO_AT 32
#define H1_DIRECT 4
#define TPAT 4
#define PACE_R 550
#define PACE_A 90
#define QG_AT 6u
#define H1_SLEEP 1
#define H2_SLEEP 1
#define ML_SLEEP 8
constexpr int SLOT = 16384;
constexpr int L_RING = 0, L_A = 8 * SLOT, L_B = L_A + SLOT, L_Q = L_B + 8192, L_HB = L_Q + 1024, L_ML = L_HB + 2048, L_CTRL = L_ML + 256, L_BYTES = L_CTRL + 256;
constexpr int STG_ROW = 272;
constexpr int HB_ROW = 144;
constexpr int C_LANDED = 0, C_GUFREE = 1, C_QRDY = 2, C_SEEN = 3, C_DONE = 4, C_MRG = 8, C_STG = 12, C_TAG = 16, C_TAGOK = 18, C_WOK = 19, C_CLEAN = 20, C_G = 24;
static_assert(L_BYTES <= 160 * 1024, "LDS");
constexpr size_t W_EPOCH = 0, W_G = 256, W_FLAG = 4096;
constexpr size_t W_QG = 8192;
constexpr size_t W_GP = W_QG + (size_t)2 * NB * 4096 * 8;
constexpr size_t W_HG = W_GP + (size_t)224 * 2048;
constexpr size_t W_PG = W_HG + (size_t)NB * FF * 8;
constexpr unsigned PGB = 16384 + 4096;
constexpr size_t W_OG = W_PG + (size_t)HQ * PGB;
constexpr size_t W_END = W_OG + (size_t)NWG * 4096;
static_assert(W_END < (1u << 30) && W_PG % 1024 == 0, "buffer offsets are int");
constexpr u64 EPOCH_STEP = 0x9E3779B97F4A7C15ull;
constexpr unsigned GSPIN = 1u << 15, LSPIN = 1u << 18;
constexpr float SC2 = 0.08838834764831845f * 1.4426950408889634f;

struct Args {
  const unsigned char *x, *k, *v, *wq, *wo, *wg, *wu, *wd, *gw;
  unsigned char *out, *ws;
};

__device__ __forceinline__ f32x4 mfma(v4u a, v4u b, f32x4 c) {
  return __builtin_amdgcn_mfma_f32_16x16x32_bf16(__builtin_bit_cast(bf16x8, a), __builtin_bit_cast(bf16x8, b), c, 0, 0, 0);
}
__device__ __forceinline__ v2u trread(LAS unsigned char* p) {
  return __builtin_bit_cast(v2u, __builtin_amdgcn_ds_read_tr16_b64_v4i16((LAS s16x4*)p));
}
__device__ __forceinline__ unsigned cvtpk(float lo, float hi) {
  typedef __bf16 bf16x2 __attribute__((ext_vector_type(2)));
  bf16x2 r; r[0] = (__bf16)lo; r[1] = (__bf16)hi; return __builtin_bit_cast(unsigned, r);
}
template <int CTRL> __device__ __forceinline__ float dpp(float x) {
  return __builtin_bit_cast(float, __builtin_amdgcn_mov_dpp(__builtin_bit_cast(int, x), CTRL, 0xf, 0xf, true));
}
__device__ __forceinline__ float xrow_max(float x) {
  auto s = __builtin_amdgcn_permlane16_swap(__float_as_uint(x), __float_as_uint(x), false, false);
  x = fmaxf(__uint_as_float(s[0]), __uint_as_float(s[1]));
  auto t = __builtin_amdgcn_permlane32_swap(__float_as_uint(x), __float_as_uint(x), false, false);
  return fmaxf(__uint_as_float(t[0]), __uint_as_float(t[1]));
}
__device__ __forceinline__ float xrow_sum(float x) {
  auto s = __builtin_amdgcn_permlane16_swap(__float_as_uint(x), __float_as_uint(x), false, false);
  x = __uint_as_float(s[0]) + __uint_as_float(s[1]);
  auto t = __builtin_amdgcn_permlane32_swap(__float_as_uint(x), __float_as_uint(x), false, false);
  return __uint_as_float(t[0]) + __uint_as_float(t[1]);
}
__device__ __forceinline__ float bflo(unsigned u) { return __uint_as_float(u << 16); }
__device__ __forceinline__ float bfhi(unsigned u) { return __uint_as_float(u & 0xffff0000u); }

__device__ __forceinline__ v4u ld16(__amdgpu_buffer_rsrc_t rs, unsigned off) { return __builtin_amdgcn_raw_buffer_load_b128(rs, (int)off, 0, 16); }
__device__ __forceinline__ void st16(__amdgpu_buffer_rsrc_t rs, unsigned off, v4u v) { __builtin_amdgcn_raw_buffer_store_b128(v, rs, (int)off, 0, 16); }
__device__ __forceinline__ unsigned f24(float v) { return (__float_as_uint(v) + 0x80u) >> 8; }
#define TAG_LO ((unsigned)T64 << 24)
#define TAG_HI ((unsigned)(T64 >> 8))
#define TAG_OK(lo, hi) ((hi) == TAG_HI && ((lo) ^ TAG_LO) < 0x1000000u)
#define CBAR() asm volatile("" ::: "memory")
#define LDS_WAIT() asm volatile("s_waitcnt lgkmcnt(0)" ::: "memory")
#define KPOL " nt"
#define VPOL " nt"
#define WPOL " nt"
#define DMA4(sbase, dst, v0, v1, v2, v3, POL)                                                                    \
  asm volatile("s_mov_b32 m0, %5\n\ts_nop 0\n\t"                                                                 \
               "global_load_lds_dwordx4 %0, %4" POL "\n\t"                                                       \
               "global_load_lds_dwordx4 %1, %4 offset:1024" POL "\n\t"                                           \
               "global_load_lds_dwordx4 %2, %4 offset:2048" POL "\n\t"                                           \
               "global_load_lds_dwordx4 %3, %4 offset:3072" POL                                                  \
               :: "v"(v0), "v"(v1), "v"(v2), "v"(v3), "s"(sbase), "s"(dst) : "memory")
#define DMA1S(sbase, voff, dst)                                                                                  \
  asm volatile("s_mov_b32 m0, %2\n\ts_nop 0\n\tglobal_load_lds_dwordx4 %0, %1" WPOL :: "v"(voff), "s"(sbase), "s"(dst) : "memory")
#define DMA1V(vaddr, dst)                                                                                        \
  asm volatile("s_mov_b32 m0, %1\n\ts_nop 0\n\tglobal_load_lds_dwordx4 %0, off" WPOL :: "v"(vaddr), "s"(dst) : "memory")
#define VMWAIT(n) asm volatile("s_waitcnt vmcnt(" #n ")" ::: "memory")
#define VMWAITN(n) asm volatile("s_waitcnt vmcnt(%0)" :: "i"(n) : "memory")
#define TS(i) do { } while (0)
#define TS_FLUSH(w) do { } while (0)

__device__ __forceinline__ void lds_wait_ge(volatile LAS unsigned* w, unsigned need) {
#pragma unroll 1
  for (unsigned sp = 0; *w < need && sp < LSPIN; ++sp) __builtin_amdgcn_s_sleep(1);
  CBAR();
}

__global__ void __launch_bounds__(NTHR, 2) fused_kernel(Args a) {
  extern __shared__ __attribute__((aligned(16))) unsigned char smem[];
  LAS unsigned char* L = (LAS unsigned char*)smem;
  volatile LAS unsigned* CT = (volatile LAS unsigned*)(L + L_CTRL);
  const int tid = threadIdx.x, lane = tid & 63, wave = __builtin_amdgcn_readfirstlane(tid >> 6);
  const int bid = blockIdx.x;
  const int kap = bid >> 5, jg = bid & 31, b = jg >> 2, s = jg & 3;
  const int qcol = kap * 512 + (((jg & 7) << 1) | ((jg >> 3) & 1)) * 32, qk = jg >> 4;
  const bool has_ffn = (bid >> 3) < 28;
  const int fi = has_ffn ? (bid >> 3) : 27;
  const int fcg = (bid & 7) * 7 + (fi >> 2), fk = fi & 3;
  const int hh = jg >> 3, ht = kap * 4 + hh, n8 = jg & 7;
  const int g = lane >> 4, r = lane & 15;
  gu64* ws64 = (gu64*)a.ws;
  const __amdgpu_buffer_rsrc_t rs = __builtin_amdgcn_make_buffer_rsrc((void*)a.ws, 0, (int)W_END, 0x00020000);
  if (__builtin_expect(wave == 0, 1)) {
    __builtin_amdgcn_s_waitcnt(0);
    __builtin_amdgcn_s_setprio(3);
    const unsigned lb = (unsigned)(size_t)L;
    {
      const unsigned voff = (unsigned)(lane >> 2) * 8192u + (unsigned)(lane & 3) * 16u;
      const unsigned char* sb = a.wq + (size_t)qk * 256 * 8192 + (size_t)qcol * 2;
#pragma unroll 1
      for (int j = 0; j < 16; ++j) DMA1S(sb + (size_t)j * 16 * 8192, voff, lb + L_A + j * 1024);
    }
    {
      const unsigned char* p = ((lane & 32) ? a.wu : a.wg) + (size_t)(fk * 128 + ((lane >> 2) & 7)) * (FF * 2) + (size_t)fcg * 64 + (lane & 3) * 16;
#pragma unroll 1
      for (int j = 0; j < 16; ++j) DMA1V(p + (size_t)j * 8 * (FF * 2), lb + L_RING + 7 * SLOT + j * 1024);
    }
    __builtin_amdgcn_s_barrier();
    CBAR();
    unsigned ko[4], vo[2];
#pragma unroll
    for (int j = 0; j < 4; ++j) ko[j] = (unsigned)g * 256u + (unsigned)((r ^ g ^ (4 * j)) * 16);
#pragma unroll
    for (int j = 0; j < 2; ++j) vo[j] = (unsigned)g * 256u + (unsigned)((r ^ (2 * (4 * j + g))) * 16);
    const size_t seq_off = (size_t)(b * HKV + kap) * SK * 256;
    const unsigned char* kb = a.k + seq_off;
    const unsigned char* vb = a.v + seq_off;
#define LOAD_WO(last) do { \
      const unsigned voff = (unsigned)(lane >> 3) * 1024u + (unsigned)(lane & 7) * 16u; \
      const unsigned char* sb = a.wo + (size_t)ht * 128 * 1024 + (size_t)n8 * 128; \
      const unsigned char* sd = a.wd + (size_t)ht * 56 * 1024 + (size_t)n8 * 128; \
      _Pragma("unroll 1") for (int j = 0; j < 23; ++j) { \
        DMA1S(j < 16 ? sb + (size_t)j * 8192 : sd + (size_t)(j - 16) * 8192, voff, lb + L_A + j * 1024); \
        if (last) { if (j == 7) { VMWAIT(8); CT[C_LANDED] = 2u + 2u * NTILE; } } \
        else if ((j & 7) == 7) VMWAIT(16); \
      } } while (0)
    unsigned seen = 0;
#define PACE_SK 0
    const unsigned pace0 = (unsigned)__builtin_amdgcn_s_memrealtime() - (unsigned)((bid - 128) * PACE_SK / 256);
    bool deep = true;
#pragma unroll 1
    for (int n = 0; n < NTILE; ++n) {
      const int tile = ((n + 4 * b) & 31) * 4 + s;
      const int cs = n & 3;
      if (n == WO_AT) LOAD_WO(false);
      if (n == WO_AT + 2) CT[C_WOK] = 1u;
#pragma unroll
      for (int hv = 0; hv < 2; ++hv) {
        const unsigned f = 2u + 2u * n + hv;
        if (n >= 4 || (n == 3 && hv == 1)) {
          volatile LAS unsigned* fw = n >= 4 ? (CT + C_DONE + cs) : (CT + C_GUFREE);
          const unsigned need = n >= 4 ? (unsigned)(2 * (n >> 2) - 1 + hv) : 1u;
          if (n < 4 || seen < need) {
            if (*fw < need) {
              VMWAIT(0); CT[C_LANDED] = f;
              lds_wait_ge(fw, need);
            }
          }
        }
        const unsigned char* sb = (hv ? vb : kb) + (size_t)tile * SLOT;
        const unsigned dst = lb + L_RING + (hv * 4 + cs) * SLOT;
        if (hv == 0) { DMA4(sb, dst, ko[0], ko[1], ko[2], ko[3], KPOL); DMA4(sb + 4096, dst + 4096, ko[0], ko[1], ko[2], ko[3], KPOL); }
        else         { DMA4(sb, dst, vo[0], vo[1], vo[0], vo[1], VPOL); DMA4(sb + 4096, dst + 4096, vo[0], vo[1], vo[0], vo[1], VPOL); }
        if (deep) VMWAIT(32); else VMWAIT(16);
        if (hv == 0) { DMA4(sb + 8192, dst + 8192, ko[0], ko[1], ko[2], ko[3], KPOL); DMA4(sb + 12288, dst + 12288, ko[0], ko[1], ko[2], ko[3], KPOL); }
        else         { DMA4(sb + 8192, dst + 8192, vo[0], vo[1], vo[0], vo[1], VPOL); DMA4(sb + 12288, dst + 12288, vo[0], vo[1], vo[0], vo[1], VPOL); }
        const unsigned nx = CT[C_DONE + (hv ? ((cs + 1) & 3) : cs)];
        const unsigned now = (unsigned)__builtin_amdgcn_s_memrealtime() - pace0;
        if (deep) { VMWAIT(32); CT[C_LANDED] = f - 1; } else { VMWAIT(16); CT[C_LANDED] = f; }
        deep = now > (unsigned)(PACE_A + PACE_R * (int)f / 10 + (n >= WO_AT ? 80 : 0)) && f < 2u * NTILE;
        seen = __builtin_amdgcn_readfirstlane(nx);
        if (f == 12) TS(1);
        if (f == 3) TS(5);
        if (f == 6) TS(6);
        if (f == 34) TS(7);
      }
    }
    {
      if (WO_AT < NTILE) {
        VMWAIT(16); CT[C_LANDED] = 1u + 2u * NTILE;
        VMWAIT(0); CT[C_LANDED] = 2u + 2u * NTILE;
        TS(2);
      } else {
        VMWAIT(16); CT[C_LANDED] = 1u + 2u * NTILE;
        LOAD_WO(true);
        TS(2);
        VMWAIT(0);
      }
      CT[C_WOK] = 1u;
      TS(3);
      TS_FLUSH(0);
    }
    return;
  }

  const int c = wave - 1;
  u64 T64 = 0;
  v4u xf[8];
  if (wave == 5) T64 = __hip_atomic_load(ws64 + W_EPOCH / 8, RLX_AGENT);
  if (wave <= 2) {
    const GAS v4u* xp = (const GAS v4u*)(a.x + (size_t)(r & 7) * 1024 + (wave == 1 ? qk * 512 : fk * 256) + g * 16);
#pragma unroll
    for (int ks = 0; ks < 8; ++ks) xf[ks] = xp[(wave == 1 ? ks : (ks & 3)) * 4];
  }
  if (wave == 3) ((LAS unsigned*)(L + L_CTRL))[lane] = 0u;
  if (wave == 4) ((LAS v4u*)(L + L_B + 7168))[lane] = v4u{0u, 0u, 0u, 0u};
  LDS_WAIT();
  __builtin_amdgcn_s_barrier();
  CBAR();

  if (wave == 5) {
    T64 += EPOCH_STEP;
    if (lane == 0) { CT[C_TAG] = (unsigned)T64; CT[C_TAG + 1] = (unsigned)(T64 >> 32); LDS_WAIT(); CT[C_TAGOK] = 1u; }
    if (bid == NWG - 1) {
      const int gb = lane >> 3, part = lane & 7;
      const GAS v4u* xp = (const GAS v4u*)(a.x + (size_t)gb * 1024 + part * 128);
      const GAS v4u* gp = (const GAS v4u*)(a.gw + part * 128);
      float ga = 0.f;
#pragma unroll
      for (int i = 0; i < 8; ++i) {
        const v4u xv = xp[i], gv = gp[i];
#pragma unroll
        for (int e = 0; e < 4; ++e) { ga = fmaf(bflo(xv[e]), bflo(gv[e]), ga); ga = fmaf(bfhi(xv[e]), bfhi(gv[e]), ga); }
      }
      ga += dpp<0xB1>(ga); ga += dpp<0x4E>(ga); ga += dpp<0x141>(ga);
      const float gate = 1.f / (1.f + __expf(-ga));
      if (part == 0) __hip_atomic_store(ws64 + W_G / 8 + gb, ((u64)TAG_HI << 32) | (f24(gate) | TAG_LO), RLX_AGENT);
    }
    {
      lds_wait_ge(CT + C_LANDED, QG_AT);
      const unsigned qo = (unsigned)W_QG + (unsigned)((b * HKV + kap) * 8192 + lane * 16);
      _Pragma("unroll 1") for (unsigned sp = 0; sp < GSPIN; ++sp) {
        v4u v[8]; bool ok = true;
#pragma unroll
        for (int i = 0; i < 8; ++i) v[i] = ld16(rs, qo + i * 1024u);
#pragma unroll
        for (int i = 0; i < 4; ++i) {
          ok &= TAG_OK(v[i].x, v[i].y) && TAG_OK(v[i].z, v[i].w) && TAG_OK(v[i + 4].x, v[i + 4].y) && TAG_OK(v[i + 4].z, v[i + 4].w);
          ((LAS unsigned*)(L + L_Q))[i * 64 + lane] = cvtpk(__uint_as_float(v[i].x << 8) + __uint_as_float(v[i + 4].x << 8), __uint_as_float(v[i].z << 8) + __uint_as_float(v[i + 4].z << 8));
        }
        if (__all(ok)) break;
        __builtin_amdgcn_s_sleep(8);
      }
      LDS_WAIT(); CT[C_QRDY] = 1u;
      TS(2);
    }
    {
      lds_wait_ge(CT + C_LANDED, 12u);
      const int fb = lane < 32 ? kap * 32 + lane : (bid & 7) + 8 * ((fi & ~3) + (lane & 3));
      _Pragma("unroll 1") for (unsigned sp = 0; sp < GSPIN; ++sp) {
        if (__all(__hip_atomic_load(ws64 + W_FLAG / 8 + fb, RLX_AGENT) == T64)) break;
        __builtin_amdgcn_s_sleep(32);
      }
      CBAR();
    }
    if (has_ffn) {
      const unsigned go = (unsigned)W_GP + (unsigned)((fcg * 4 + fk) * 2048 + lane * 16);
      const f32x4 p0 = __builtin_bit_cast(f32x4, ld16(rs, go)), p1 = __builtin_bit_cast(f32x4, ld16(rs, go + 1024u));
      unsigned hw[4];
#pragma unroll
      for (int e = 0; e < 4; ++e) {
        const unsigned t = __float_as_uint(p0[e] + p1[e]);
        auto s32 = __builtin_amdgcn_permlane32_swap(t, t, false, false);
        const unsigned tot = __float_as_uint(__uint_as_float(s32[0]) + __uint_as_float(s32[1]));
        auto s16 = __builtin_amdgcn_permlane16_swap(tot, tot, false, false);
        const float gt = __uint_as_float(s16[0]), up = __uint_as_float(s16[1]);
        const float u = 0.7978845608028654f * (gt + 0.044715f * gt * gt * gt);
        const float th = 1.f - 2.f / (__expf(2.f * u) + 1.f);
        hw[e] = f24(0.5f * gt * (1.f + th) * up) | TAG_LO;
      }
      const int src = ((2 * (g & 1) + (r >> 3)) * 4 + ((r & 7) >> 1)) * 4;
      const unsigned a0 = __builtin_amdgcn_ds_bpermute(src, (int)hw[0]), a1 = __builtin_amdgcn_ds_bpermute(src, (int)hw[1]);
      const unsigned a2 = __builtin_amdgcn_ds_bpermute(src, (int)hw[2]), a3 = __builtin_amdgcn_ds_bpermute(src, (int)hw[3]);
      if (lane < 32)
        st16(rs, (unsigned)W_HG + (unsigned)((4 * (fk & 1) + 2 * g + (r >> 3)) * FF + fcg * 32 + (fk >> 1) * 16 + 2 * (r & 7)) * 8u,
             v4u{(r & 1) ? a2 : a0, TAG_HI, (r & 1) ? a3 : a1, TAG_HI});
    }
    {
      lds_wait_ge(CT + C_LANDED, 20u);
      const bool pad = (lane & 31) >= 28;
      const unsigned ho = (unsigned)W_HG + (unsigned)((lane >> 5) * FF + ht * 56 + (pad ? 0 : 2 * (lane & 31))) * 8u;
      _Pragma("unroll 1") for (unsigned sp = 0; sp < GSPIN; ++sp) {
        v4u v[4];
#pragma unroll
        for (int i = 0; i < 4; ++i) v[i] = ld16(rs, ho + (unsigned)(i * 2 * FF * 8));
        const u64 gv = __hip_atomic_load(ws64 + W_G / 8 + (lane & 7), RLX_AGENT);
        bool ok = TAG_OK((unsigned)gv, (unsigned)(gv >> 32));
#pragma unroll
        for (int i = 0; i < 4; ++i) {
          ok &= TAG_OK(v[i].x, v[i].y) && TAG_OK(v[i].z, v[i].w);
          *(LAS unsigned*)(L + L_HB + (2 * i + (lane >> 5)) * HB_ROW + (lane & 31) * 4) =
              pad ? 0u : cvtpk(__uint_as_float(v[i].x << 8), __uint_as_float(v[i].z << 8));
        }
        if (lane < 8) CT[C_G + lane] = (unsigned)gv << 8;
        if (__all(ok)) break;
        __builtin_amdgcn_s_sleep(32);
      }
      LDS_WAIT();
      CT[C_CLEAN] = 1u;
      TS(1);
    }
    TS(3);
    {
      const unsigned oo = (unsigned)W_OG + (unsigned)(bid * 4096), ol = (unsigned)lane * 16u;
      float t0[4], t1[4];
      v4u va[4], vb[4];
      unsigned need = 15u, ca, cb;
#define H2_ISSUE(v, cm) do { cm = need; _Pragma("unroll") for (int i = 0; i < 4; ++i) v[i] = ld16(rs, ((cm >> i) & 1u) ? oo + i * 1024u + ol : (unsigned)W_QG + (unsigned)bid * 2048u); } while (0)
#define H2_CHECK(v, cm) do { _Pragma("unroll") for (int i = 0; i < 4; ++i) if (((cm & need) >> i) & 1u) { \
        if (__all(TAG_OK(v[i].x, v[i].y) && TAG_OK(v[i].z, v[i].w))) { t0[i] = __uint_as_float(v[i].x << 8); t1[i] = __uint_as_float(v[i].z << 8); need &= ~(1u << i); } } } while (0)
#pragma unroll 1
      for (unsigned sp = 0; sp < LSPIN; ++sp) { const v4u fl = *(volatile LAS v4u*)(CT + C_STG); if (fl.x & fl.y & fl.z & fl.w) break; __builtin_amdgcn_s_sleep(1); }
      H2_ISSUE(va, ca);
      _Pragma("unroll 1") for (unsigned sp = 0; sp < GSPIN; ++sp) {
        __builtin_amdgcn_s_sleep(H2_SLEEP);
        H2_ISSUE(vb, cb);
        H2_CHECK(va, ca);
        if (need == 0u) break;
        __builtin_amdgcn_s_sleep(H2_SLEEP);
        H2_ISSUE(va, ca);
        H2_CHECK(vb, cb);
        if (need == 0u) break;
      }
      float s0 = (t0[0] + t0[1]) + (t0[2] + t0[3]), s1 = (t1[0] + t1[1]) + (t1[2] + t1[3]);
      s0 += dpp<0x128>(s0); s1 += dpp<0x128>(s1);
      s0 = xrow_sum(s0); s1 = xrow_sum(s1);
      if (lane < 8) ((GAS unsigned*)a.out)[bid * 8 + lane] = cvtpk(s0, s1);
      TS(4);
      if (bid == 0 && lane == 0) __hip_atomic_store(ws64 + W_EPOCH / 8, T64, RLX_AGENT);
    }
    TS_FLUSH(5);
    return;
  }

  if (c == 0) {
    lds_wait_ge(CT + C_TAGOK, 1u);
    T64 = ((u64)CT[C_TAG + 1] << 32) | CT[C_TAG];
    lds_wait_ge(CT + C_LANDED, 1u);
    TS(0);
    LAS unsigned char* sl = L + L_A + (8 * g + (r >> 2)) * 64 + (r & 3) * 8;
    f32x4 d[2] = {{0.f, 0.f, 0.f, 0.f}, {0.f, 0.f, 0.f, 0.f}};
#pragma unroll
    for (int ks = 0; ks < 8; ++ks)
#pragma unroll
      for (int ct = 0; ct < 2; ++ct) {
        const v2u b0 = trread(sl + ks * 2048 + ct * 32), b1 = trread(sl + ks * 2048 + ct * 32 + 256);
        d[ct] = mfma(xf[ks], v4u{b0.x, b0.y, b1.x, b1.y}, d[ct]);
      }
    const f32x4 dv = g < 2 ? d[0] : d[1];
    gu64* qg = ws64 + W_QG / 8 + (size_t)((4 * (g & 1) * HKV + kap) * 2 + qk) * 512 + (qcol & 511) + (g >> 1) * 16 + r;
#pragma unroll
    for (int e = 0; e < 4; ++e) __hip_atomic_store(qg + (size_t)e * HKV * 1024, ((u64)TAG_HI << 32) | (f24(dv[e]) | TAG_LO), RLX_AGENT);
    TS(1);
  } else if (c == 1) {
    lds_wait_ge(CT + C_TAGOK, 1u);
    T64 = ((u64)CT[C_TAG + 1] << 32) | CT[C_TAG];
    const v4u inv = {0u, ~(unsigned)T64, 0u, ~(unsigned)T64};
    const unsigned co = (unsigned)W_PG + (unsigned)(kap * 4 + (lane >> 5)) * PGB + (unsigned)(jg * 512 + (lane & 31) * 16);
    st16(rs, co, inv);
    st16(rs, co + 2u * PGB, inv);
    lds_wait_ge(CT + C_LANDED, 2u);
    LAS unsigned char* sl = L + L_RING + 7 * SLOT + g * 1024 + (r >> 2) * 64 + (r & 3) * 8;
    f32x4 d[4] = {{0.f, 0.f, 0.f, 0.f}, {0.f, 0.f, 0.f, 0.f}, {0.f, 0.f, 0.f, 0.f}, {0.f, 0.f, 0.f, 0.f}};
#pragma unroll
    for (int ks = 0; ks < 4; ++ks)
#pragma unroll
      for (int mc = 0; mc < 4; ++mc) {
        LAS unsigned char* q = sl + ks * 4096 + (mc >> 1) * 512 + (mc & 1) * 32;
        const v2u b0 = trread(q), b1 = trread(q + 256);
        d[mc] = mfma(xf[ks], v4u{b0.x, b0.y, b1.x, b1.y}, d[mc]);
      }
    LDS_WAIT();
    CT[C_GUFREE] = 1u;
    if (has_ffn) {
      const f32x4 dg = g < 2 ? d[0] : d[1], du = g < 2 ? d[2] : d[3];
      GAS unsigned* gp = (GAS unsigned*)(a.ws + W_GP + (size_t)(fcg * 4 + 2 * (g >> 1) + (g & 1)) * 2048 + fk * 512) + r;
#pragma unroll
      for (int e = 0; e < 4; ++e) {
        __hip_atomic_store(gp + e * 16, __float_as_uint(dg[e]), RLX_AGENT);
        __hip_atomic_store(gp + 64 + e * 16, __float_as_uint(du[e]), RLX_AGENT);
      }
    }
    VMWAIT(0);
    if (lane == 0) __hip_atomic_store(ws64 + W_FLAG / 8 + bid, T64, RLX_AGENT);
  } else {
    lds_wait_ge(CT + C_TAGOK, 1u);
    T64 = ((u64)CT[C_TAG + 1] << 32) | CT[C_TAG];
  }
  const unsigned T32 = (unsigned)T64;
  const v4u ones = {0x3F803F80u, 0x3F803F80u, 0x3F803F80u, 0x3F803F80u};
  LAS unsigned char* kbase = L + L_RING + c * SLOT + r * 256;
  LAS unsigned char* vbase = L + L_RING + (4 + c) * SLOT + (4 * g + (r >> 2)) * 256 + (r & 3) * 8;
  const int vx = 4 * (g & 1) + (r >> 2);

  v4u qf[4];
  f32x4 acc[8], accl = {0.f, 0.f, 0.f, 0.f};
#pragma unroll
  for (int m = 0; m < 8; ++m) acc[m] = f32x4{0.f, 0.f, 0.f, 0.f};
  float mrun = -INFINITY, fown = 0.f;
  v4u mlw = {0u, 0u, 0u, 0u};
  f32x4 basea = {0.f, 0.f, 0.f, 0.f}, baseb = {0.f, 0.f, 0.f, 0.f};
#pragma unroll 1
  for (int lap = 0; lap < NTILE / 4; ++lap) {
    const unsigned n = 4u * lap + c;
    lds_wait_ge(CT + C_LANDED, 3u + 2u * n);
    v4u kf[4][4];
#pragma unroll
    for (int T = 0; T < 4; ++T)
#pragma unroll
      for (int ks = 0; ks < 4; ++ks) kf[T][ks] = *(const LAS v4u*)(kbase + T * 4096 + (((4 * ks + g) ^ r) * 16));
    LDS_WAIT();
    CT[C_DONE + c] = (unsigned)(2 * lap + 1);
#pragma unroll
    for (int T = 0; T < 4; ++T) asm volatile("" : "+v"(kf[T][0]), "+v"(kf[T][1]), "+v"(kf[T][2]), "+v"(kf[T][3]) :: "memory");
    v4u vf[2][8];
#define READ_V() do { \
      lds_wait_ge(CT + C_LANDED, 4u + 2u * n); \
      _Pragma("unroll") for (int ks = 0; ks < 2; ++ks) \
        _Pragma("unroll") for (int m = 0; m < 8; ++m) { \
          LAS unsigned char* p = vbase + ks * 8192 + ((m ^ vx) * 32); \
          const v2u t0 = trread(p), t1 = trread(p + 4096); \
          vf[ks][m] = v4u{t0.x, t0.y, t1.x, t1.y}; \
        } \
      LDS_WAIT(); \
      CT[C_DONE + c] = (unsigned)(2 * lap + 2); \
      _Pragma("unroll") for (int ks = 0; ks < 2; ++ks) { \
        asm volatile("" : "+v"(vf[ks][0]), "+v"(vf[ks][1]), "+v"(vf[ks][2]), "+v"(vf[ks][3]) :: "memory"); \
        asm volatile("" : "+v"(vf[ks][4]), "+v"(vf[ks][5]), "+v"(vf[ks][6]), "+v"(vf[ks][7]) :: "memory"); \
      } } while (0)
    if (lap == 0) {
      READ_V();
      lds_wait_ge(CT + C_QRDY, 1u);
#pragma unroll
      for (int ks = 0; ks < 4; ++ks) qf[ks] = *(const LAS v4u*)(L + L_Q + ((r >> 2) * 128 + 32 * ks + 8 * g) * 2);
    }
    f32x4 sc[4];
#pragma unroll
    for (int T = 0; T < 4; ++T) {
      f32x4 d = {0.f, 0.f, 0.f, 0.f};
#pragma unroll
      for (int ks = 0; ks < 4; ++ks) d = mfma(kf[T][ks], qf[ks], d);
      sc[T] = d;
    }
    float tm = fmaxf(fmaxf(sc[0][0], sc[0][1]), fmaxf(sc[0][2], sc[0][3]));
#pragma unroll
    for (int T = 1; T < 4; ++T) tm = fmaxf(tm, fmaxf(fmaxf(sc[T][0], sc[T][1]), fmaxf(sc[T][2], sc[T][3])));
    tm = xrow_max(tm);
    if (__any(tm > mrun)) {
      const float mn = fmaxf(mrun, tm);
      const float al = __builtin_amdgcn_exp2f((mrun - mn) * SC2);
#pragma unroll
      for (int m = 0; m < 8; ++m) acc[m] *= al;
      accl *= al;
      mrun = mn;
    }
    const float mo = -mrun * SC2;
    v4u pf[2];
#pragma unroll
    for (int T = 0; T < 4; ++T) {
      float p[4];
#pragma unroll
      for (int e = 0; e < 4; ++e) p[e] = __builtin_amdgcn_exp2f(fmaf(sc[T][e], SC2, mo));
      pf[T >> 1][(T & 1) * 2] = cvtpk(p[0], p[1]);
      pf[T >> 1][(T & 1) * 2 + 1] = cvtpk(p[2], p[3]);
    }
    accl = mfma(ones, pf[0], accl);
    accl = mfma(ones, pf[1], accl);
    asm volatile("" : "+v"(pf[0]), "+v"(pf[1]), "+v"(accl) :: "memory");
    if (n == NTILE - 1) {
      float mw[3], lw[3]; f32x4 oa[3], ob[3];
#pragma unroll
      for (int w = 0; w < 3; ++w) {
        lds_wait_ge(CT + C_MRG + w, 1u);
        const LAS float* rw = (const LAS float*)(L + L_RING + w * SLOT);
        mw[w] = rw[512 + (r >> 2)]; lw[w] = rw[516 + (r >> 2)];
        oa[w] = *(const LAS f32x4*)(rw + r * 32 + 4 * g); ob[w] = *(const LAS f32x4*)(rw + r * 32 + 16 + 4 * g);
      }
      const unsigned m24 = f24(fmaxf(fmaxf(mrun, mw[0]), fmaxf(mw[1], mw[2])) * SC2);
      const float Mr = __uint_as_float(m24 << 8);
      fown = __builtin_amdgcn_exp2f(fmaf(mrun, SC2, -Mr));
      float Ls = fown * accl[0];
#pragma unroll
      for (int w = 0; w < 3; ++w) {
        const float f = __builtin_amdgcn_exp2f(fmaf(mw[w], SC2, -Mr));
        Ls = fmaf(f, lw[w], Ls); basea += f * oa[w]; baseb += f * ob[w];
      }
      const float inv = 1.f / Ls;
      fown *= inv; basea *= inv; baseb *= inv;
      mlw = v4u{m24 | TAG_LO, TAG_HI, f24(Ls) | TAG_LO, TAG_HI};
    }
    if (lap) READ_V();
#pragma unroll
    for (int ks = 0; ks < 2; ++ks)
#pragma unroll
      for (int m = 0; m < 8; ++m) acc[m] = mfma(vf[ks][m], pf[ks], acc[m]);
  }
  TS(4);
  if (c == 3) {
    const int u = r & 3;
    const f32x4 sa = u == 0 ? acc[0] : u == 1 ? acc[2] : u == 2 ? acc[4] : acc[6], sb = u == 0 ? acc[1] : u == 1 ? acc[3] : u == 2 ? acc[5] : acc[7];
    const f32x4 xa = fown * sa + basea, xb = fown * sb + baseb;
    const unsigned po = (unsigned)W_PG + (unsigned)(kap * 4 + (r >> 2)) * PGB + (unsigned)(jg * 512 + g * 64 + u * 16);
    const v2u pa = {cvtpk(xa[0], xa[1]), cvtpk(xa[2], xa[3])}, pb = {cvtpk(xb[0], xb[1]), cvtpk(xb[2], xb[3])};
    st16(rs, po, v4u{pa.x, T32, pa.y, T32});
    st16(rs, po + 256u, v4u{pb.x, T32, pb.y, T32});
    if ((lane & 51) == 0) st16(rs, (unsigned)W_PG + (unsigned)(kap * 4 + (r >> 2)) * PGB + (unsigned)(16384 + jg * 128), mlw);
    if ((r >> 2) == hh) {
      LAS unsigned char* own = L + L_RING + (jg >> 3) * SLOT + 4096 + (jg & 7) * STG_ROW + 64 * u + 8 * g;
      *(LAS v2u*)own = pa; *(LAS v2u*)(own + 32) = pb;
      if ((lane & 51) == 0) *(LAS v2u*)(L + L_ML + jg * 8) = v2u{mlw.x << 8, mlw.z << 8};
    }
    CT[C_SEEN] = 1u;
    TS(2);
  } else {
    LAS float* rec = (LAS float*)(L + L_RING + c * SLOT);
    if ((r & 3) == 0) {
#pragma unroll
      for (int m = 0; m < 8; ++m) *(LAS f32x4*)(rec + (r >> 2) * 128 + 16 * m + 4 * g) = acc[m];
      if (g == 0) { rec[512 + (r >> 2)] = mrun; rec[516 + (r >> 2)] = accl[0]; }
    }
    LDS_WAIT();
    CT[C_MRG + c] = 1u;
  }
  lds_wait_ge(CT + C_CLEAN, 1u);
  {
    LAS unsigned* stw = (LAS unsigned*)(L + L_RING + c * SLOT + 4096) + 16 * ((lane >> 1) & 3) + 8 * (lane >> 5) + 2 * ((lane >> 3) & 3) + (lane & 1);
    gu64* pgp = (gu64*)(a.ws + W_PG + (size_t)ht * PGB + c * 4096);
    gu64* dmy = ws64 + W_QG / 8 + (size_t)bid * 256;
    gu64* mlp = (gu64*)(a.ws + W_PG + (size_t)ht * PGB + 16384 + (8 * c + ((lane >> 1) & 7)) * 128) + (lane & 1);
    unsigned need = c == (jg >> 3) ? 0x1ffu & ~(1u << (jg & 7)) : 0x1ffu, ca = 0u, cb = 0u;
    u64 va[9], vb[9];
#define H1_ISSUE(v, cm) do { cm = need; \
      _Pragma("unroll") for (int i = 0; i < 8; ++i) v[i] = __hip_atomic_load(((cm >> i) & 1u) ? pgp + i * 64 + lane : dmy, RLX_AGENT); \
      v[8] = __hip_atomic_load(!mlok ? mlp : dmy, RLX_AGENT); } while (0)
#define H1_CHECK(v, cm) do { _Pragma("unroll") for (int i = 0; i < 8; ++i) \
      if (((cm & need) >> i) & 1u) { if (__all((unsigned)(v[i] >> 32) == T32)) { stw[i * (STG_ROW / 4)] = (unsigned)v[i]; need &= ~(1u << i); } } \
      if (!mlok && TAG_OK((unsigned)v[8], (unsigned)(v[8] >> 32))) { ((LAS unsigned*)(L + L_ML))[16 * c + lane] = (unsigned)v[8] << 8; mlok = true; } \
      if (__all(mlok)) need &= 0xffu; } while (0)
    bool mlok = lane >= 16 || (c == (jg >> 3) && (lane >> 1) == (jg & 7));
    lds_wait_ge(CT + C_SEEN, 1u);
    H1_ISSUE(va, ca);
    _Pragma("unroll 1") for (unsigned sp = 0; sp < GSPIN; ++sp) {
      if (__builtin_popcount(need) <= H1_DIRECT) __builtin_amdgcn_s_sleep(H1_SLEEP); else __builtin_amdgcn_s_sleep(ML_SLEEP);
      H1_ISSUE(vb, cb);
      H1_CHECK(va, ca);
      if (need == 0u) break;
      if (__builtin_popcount(need) <= H1_DIRECT) __builtin_amdgcn_s_sleep(H1_SLEEP); else __builtin_amdgcn_s_sleep(ML_SLEEP);
      H1_ISSUE(va, ca);
      H1_CHECK(vb, cb);
      if (need == 0u) break;
    }
    TS(6);
    LDS_WAIT();
    CT[C_STG + c] = 1u;
    lds_wait_ge(CT + C_WOK, 1u);
    v4u bw[4]; f32x4 dff = {0.f, 0.f, 0.f, 0.f};
    {
      LAS unsigned char* wb = L + L_A + (8 * g + (r >> 2)) * 128 + c * 32 + (r & 3) * 8;
#pragma unroll
      for (int ks = 0; ks < 4; ++ks) { const v2u t0 = trread(wb + ks * 4096), t1 = trread(wb + ks * 4096 + 512); bw[ks] = v4u{t0.x, t0.y, t1.x, t1.y}; }
#pragma unroll
      for (int ks = 0; ks < 2; ++ks) {
        const v2u t0 = trread(wb + SLOT + ks * 4096), t1 = trread(wb + SLOT + ks * 4096 + 512);
        const v4u hf = *(const LAS v4u*)(L + L_HB + (4 * (r & 1) + (r >> 2)) * HB_ROW + 64 * ks + 16 * g);
        dff = mfma(hf, v4u{t0.x, t0.y, t1.x, t1.y}, dff);
      }
    }
    asm volatile("" : "+v"(bw[0]), "+v"(bw[1]), "+v"(bw[2]), "+v"(bw[3]), "+v"(dff) :: "memory");
    TS(5);
#pragma unroll 1
    for (unsigned sp = 0; sp < LSPIN; ++sp) { const v4u fl = *(volatile LAS v4u*)(CT + C_STG); if (fl.x & fl.y & fl.z & fl.w) break; }
    CBAR();
    if (c == 3) TS(3);
    const LAS float* mls = (const LAS float*)(L + L_ML);
    LAS unsigned char* stg = L + L_RING + (r >> 3) * SLOT + 4096 + (r & 7) * STG_ROW + 16 * g;
#pragma unroll
    for (int rt = 0; rt < 2; ++rt) {
      f32x4 d = {0.f, 0.f, 0.f, 0.f};
#pragma unroll
      for (int ks = 0; ks < 4; ++ks) d = mfma(*(const LAS v4u*)(stg + 2 * rt * SLOT + 64 * ks), bw[ks], d);
      const f32x4 m01 = *(const LAS f32x4*)(mls + 32 * rt + 8 * g), m23 = *(const LAS f32x4*)(mls + 32 * rt + 8 * g + 4);
      const float Mx = fmaxf(fmaxf(m01[0], m01[2]), fmaxf(m23[0], m23[2]));
      const float w0 = __builtin_amdgcn_exp2f(m01[0] - Mx) * m01[1], w1 = __builtin_amdgcn_exp2f(m01[2] - Mx) * m01[3];
      const float w2 = __builtin_amdgcn_exp2f(m23[0] - Mx) * m23[1], w3 = __builtin_amdgcn_exp2f(m23[2] - Mx) * m23[3];
      const float gate = __uint_as_float(CT[C_G + 4 * rt + g]);
      const float att = ((w0 * d[0] + w1 * d[1]) + (w2 * d[2] + w3 * d[3])) / ((w0 + w1) + (w2 + w3));
      const float t = fmaf(gate, att, (1.f - gate) * dff[rt]);
      __hip_atomic_store(ws64 + W_OG / 8 + ((size_t)((4 * rt + g) * 32 + 4 * n8 + c) * 32 + ht) * 16 + r, ((u64)TAG_HI << 32) | (f24(t) | TAG_LO), RLX_AGENT);
    }
  }
  if (c == 0) TS(2);
  TS_FLUSH(wave);
}
}

extern "C" void kernel_launch(void* const* d_in, const int* in_sizes, int n_in, void* d_out, int out_size, void* d_ws,
                              size_t ws_size, hipStream_t stream) {
  (void)in_sizes; (void)n_in; (void)out_size;
  if (ws_size < W_END) return;
  Args a;
  a.x = (const unsigned char*)d_in[0]; a.k = (const unsigned char*)d_in[1]; a.v = (const unsigned char*)d_in[2];
  a.wq = (const unsigned char*)d_in[3]; a.wo = (const unsigned char*)d_in[4];
  a.wg = (const unsigned char*)d_in[6]; a.wu = (const unsigned char*)d_in[7]; a.wd = (const unsigned char*)d_in[8];
  a.gw = (const unsigned char*)d_in[9];
  a.out = (unsigned char*)d_out; a.ws = (unsigned char*)d_ws;
  (void)hipFuncSetAttribute((const void*)fused_kernel, hipFuncAttributeMaxDynamicSharedMemorySize, L_BYTES);
  hipLaunchKernelGGL(fused_kernel, dim3(NWG), dim3(NTHR), L_BYTES, stream, a);
}
```

```cpp
#include <hip/hip_runtime.h>
#include <hip/hip_bf16.h>
#include <stdint.h>

#define GAS __attribute__((address_space(1)))
#define LAS __attribute__((address_space(3)))
typedef unsigned v4u __attribute__((ext_vector_type(4)));
typedef unsigned v2u __attribute__((ext_vector_type(2)));
typedef float f32x4 __attribute__((ext_vector_type(4)));
typedef float f32x2 __attribute__((ext_vector_type(2)));
typedef __bf16 bf16x8 __attribute__((ext_vector_type(8)));
typedef short s16x4 __attribute__((ext_vector_type(4)));
typedef unsigned long long u64;
typedef GAS u64 gu64;
#define RLX_AGENT __ATOMIC_RELAXED, __HIP_MEMORY_SCOPE_AGENT

namespace {
constexpr int NB = 8, DM = 512, HQ = 32, HKV = 8, SK = 8192, DH = 128, FF = 1792;
constexpr int NWG = 256, NTHR = 6 * 64;
constexpr int NTILE = 32;
#define SW 16
#define WO_AT 32
#define H1_DIRECT 4
#define TPAT 4
#define PACE_R 550
#define PACE_A 90
#define PACE_D 44
#define PACE_G0 0.5f
#define H1_GO 170
#define H2_GO 0
#define QG_AT 6u
#define H1_SLEEP 1
#define H2_SLEEP 1
#define ML_SLEEP 8
constexpr int SLOT = 16384;
constexpr int L_RING = 0, L_A = 8 * SLOT, L_B = L_A + SLOT, L_Q = L_B + 8192, L_HB = L_Q + 1024, L_ML = L_HB + 2048, L_CTRL = L_ML + 256, L_BYTES = L_CTRL + 256;
constexpr int STG_ROW = 272;
constexpr int HB_ROW = 144;
constexpr int C_LANDED = 0, C_GUFREE = 1, C_QRDY = 2, C_SEEN = 3, C_DONE = 4, C_MRG = 8, C_STG = 12, C_TAG = 16, C_TAGOK = 18, C_WOK = 19, C_CLEAN = 20, C_LAG = 21, C_PB = 22, C_PR = 23, C_G = 24, C_P0 = 32, C_TGO = 33;
static_assert(L_BYTES <= 160 * 1024, "LDS");
constexpr size_t W_EPOCH = 0, W_G = 256, W_FLAG = 4096, W_LAG = 8192;
constexpr size_t W_QG = 16384;
constexpr size_t W_GP = W_QG + (size_t)2 * NB * 4096 * 8;
constexpr size_t W_HG = W_GP + (size_t)224 * 2048;
constexpr size_t W_PG = W_HG + (size_t)NB * FF * 8;
constexpr unsigned PGB = 16384 + 4096;
constexpr size_t W_OG = W_PG + (size_t)HQ * PGB;
constexpr size_t W_END = W_OG + (size_t)NWG * 4096;
static_assert(W_END < (1u << 30) && W_PG % 1024 == 0, "buffer offsets are int");
constexpr u64 EPOCH_STEP = 0x9E3779B97F4A7C15ull;
constexpr unsigned GSPIN = 1u << 15, LSPIN = 1u << 18;
constexpr float SC2 = 0.08838834764831845f * 1.4426950408889634f;

struct Args {
  const unsigned char *x, *k, *v, *wq, *wo, *wg, *wu, *wd, *gw;
  unsigned char *out, *ws;
};

__device__ __forceinline__ f32x4 mfma(v4u a, v4u b, f32x4 c) {
  return __builtin_amdgcn_mfma_f32_16x16x32_bf16(__builtin_bit_cast(bf16x8, a), __builtin_bit_cast(bf16x8, b), c, 0, 0, 0);
}
__device__ __forceinline__ v2u trread(LAS unsigned char* p) {
  return __builtin_bit_cast(v2u, __builtin_amdgcn_ds_read_tr16_b64_v4i16((LAS s16x4*)p));
}
__device__ __forceinline__ unsigned cvtpk(float lo, float hi) {
  typedef __bf16 bf16x2 __attribute__((ext_vector_type(2)));
  bf16x2 r; r[0] = (__bf16)lo; r[1] = (__bf16)hi; return __builtin_bit_cast(unsigned, r);
}
template <int CTRL> __device__ __forceinline__ float dpp(float x) {
  return __builtin_bit_cast(float, __builtin_amdgcn_mov_dpp(__builtin_bit_cast(int, x), CTRL, 0xf, 0xf, true));
}
__device__ __forceinline__ float xrow_max(float x) {
  auto s = __builtin_amdgcn_permlane16_swap(__float_as_uint(x), __float_as_uint(x), false, false);
  x = fmaxf(__uint_as_float(s[0]), __uint_as_float(s[1]));
  auto t = __builtin_amdgcn_permlane32_swap(__float_as_uint(x), __float_as_uint(x), false, false);
  return fmaxf(__uint_as_float(t[0]), __uint_as_float(t[1]));
}
__device__ __forceinline__ float xrow_sum(float x) {
  auto s = __builtin_amdgcn_permlane16_swap(__float_as_uint(x), __float_as_uint(x), false, false);
  x = __uint_as_float(s[0]) + __uint_as_float(s[1]);
  auto t = __builtin_amdgcn_permlane32_swap(__float_as_uint(x), __float_as_uint(x), false, false);
  return __uint_as_float(t[0]) + __uint_as_float(t[1]);
}
__device__ __forceinline__ float bflo(unsigned u) { return __uint_as_float(u << 16); }
__device__ __forceinline__ float bfhi(unsigned u) { return __uint_as_float(u & 0xffff0000u); }

__device__ __forceinline__ v4u ld16(__amdgpu_buffer_rsrc_t rs, unsigned off) { return __builtin_amdgcn_raw_buffer_load_b128(rs, (int)off, 0, 16); }
__device__ __forceinline__ void st16(__amdgpu_buffer_rsrc_t rs, unsigned off, v4u v) { __builtin_amdgcn_raw_buffer_store_b128(v, rs, (int)off, 0, 16); }
__device__ __forceinline__ unsigned f24(float v) { return (__float_as_uint(v) + 0x80u) >> 8; }
#define TAG_LO ((unsigned)T64 << 24)
#define TAG_HI ((unsigned)(T64 >> 8))
#define TAG_OK(lo, hi) ((hi) == TAG_HI && ((lo) ^ TAG_LO) < 0x1000000u)
#define CBAR() asm volatile("" ::: "memory")
#define LDS_WAIT() asm volatile("s_waitcnt lgkmcnt(0)" ::: "memory")
#define KPOL " nt"
#define VPOL " nt"
#define WPOL " nt"
#define DMA4(sbase, dst, v0, v1, v2, v3, POL)                                                                    \
  asm volatile("s_mov_b32 m0, %5\n\ts_nop 0\n\t"                                                                 \
               "global_load_lds_dwordx4 %0, %4" POL "\n\t"                                                       \
               "global_load_lds_dwordx4 %1, %4 offset:1024" POL "\n\t"                                           \
               "global_load_lds_dwordx4 %2, %4 offset:2048" POL "\n\t"                                           \
               "global_load_lds_dwordx4 %3, %4 offset:3072" POL                                                  \
               :: "v"(v0), "v"(v1), "v"(v2), "v"(v3), "s"(sbase), "s"(dst) : "memory")
#define DMA1S(sbase, voff, dst)                                                                                  \
  asm volatile("s_mov_b32 m0, %2\n\ts_nop 0\n\tglobal_load_lds_dwordx4 %0, %1" WPOL :: "v"(voff), "s"(sbase), "s"(dst) : "memory")
#define DMA1V(vaddr, dst)                                                                                        \
  asm volatile("s_mov_b32 m0, %1\n\ts_nop 0\n\tglobal_load_lds_dwordx4 %0, off" WPOL :: "v"(vaddr), "s"(dst) : "memory")
#define VMWAIT(n) asm volatile("s_waitcnt vmcnt(" #n ")" ::: "memory")
#define VMWAITN(n) asm volatile("s_waitcnt vmcnt(%0)" :: "i"(n) : "memory")
#define TS(i) do { } while (0)
#define TS_FLUSH(w) do { } while (0)

__device__ __forceinline__ void lds_wait_ge(volatile LAS unsigned* w, unsigned need) {
#pragma unroll 1
  for (unsigned sp = 0; *w < need && sp < LSPIN; ++sp) __builtin_amdgcn_s_sleep(1);
  CBAR();
}

__global__ void __launch_bounds__(NTHR, 2) fused_kernel(Args a) {
  extern __shared__ __attribute__((aligned(16))) unsigned char smem[];
  LAS unsigned char* L = (LAS unsigned char*)smem;
  volatile LAS unsigned* CT = (volatile LAS unsigned*)(L + L_CTRL);
  const int tid = threadIdx.x, lane = tid & 63, wave = __builtin_amdgcn_readfirstlane(tid >> 6);
  const int bid = blockIdx.x;
  const int kap = bid >> 5, jg = bid & 31, b = jg >> 2, s = jg & 3;
  const int qcol = kap * 512 + (((jg & 7) << 1) | ((jg >> 3) & 1)) * 32, qk = jg >> 4;
  const bool has_ffn = (bid >> 3) < 28;
  const int fi = has_ffn ? (bid >> 3) : 27;
  const int fcg = (bid & 7) * 7 + (fi >> 2), fk = fi & 3;
  const int hh = jg >> 3, ht = kap * 4 + hh, n8 = jg & 7;
  const int g = lane >> 4, r = lane & 15;
  gu64* ws64 = (gu64*)a.ws;
  const __amdgpu_buffer_rsrc_t rs = __builtin_amdgcn_make_buffer_rsrc((void*)a.ws, 0, (int)W_END, 0x00020000);
  if (__builtin_expect(wave == 0, 1)) {
    __builtin_amdgcn_s_waitcnt(0);
    __builtin_amdgcn_s_setprio(3);
    const unsigned lb = (unsigned)(size_t)L;
    {
      const unsigned voff = (unsigned)(lane >> 2) * 8192u + (unsigned)(lane & 3) * 16u;
      const unsigned char* sb = a.wq + (size_t)qk * 256 * 8192 + (size_t)qcol * 2;
#pragma unroll 1
      for (int j = 0; j < 16; ++j) DMA1S(sb + (size_t)j * 16 * 8192, voff, lb + L_A + j * 1024);
    }
    {
      const unsigned char* p = ((lane & 32) ? a.wu : a.wg) + (size_t)(fk * 128 + ((lane >> 2) & 7)) * (FF * 2) + (size_t)fcg * 64 + (lane & 3) * 16;
#pragma unroll 1
      for (int j = 0; j < 16; ++j) DMA1V(p + (size_t)j * 8 * (FF * 2), lb + L_RING + 7 * SLOT + j * 1024);
    }
    __builtin_amdgcn_s_barrier();
    CBAR();
    unsigned ko[4], vo[2];
#pragma unroll
    for (int j = 0; j < 4; ++j) ko[j] = (unsigned)g * 256u + (unsigned)((r ^ g ^ (4 * j)) * 16);
#pragma unroll
    for (int j = 0; j < 2; ++j) vo[j] = (unsigned)g * 256u + (unsigned)((r ^ (2 * (4 * j + g))) * 16);
    const size_t seq_off = (size_t)(b * HKV + kap) * SK * 256;
    const unsigned char* kb = a.k + seq_off;
    const unsigned char* vb = a.v + seq_off;
#define LOAD_WO(last) do { \
      const unsigned voff = (unsigned)(lane >> 3) * 1024u + (unsigned)(lane & 7) * 16u; \
      const unsigned char* sb = a.wo + (size_t)ht * 128 * 1024 + (size_t)n8 * 128; \
      const unsigned char* sd = a.wd + (size_t)ht * 56 * 1024 + (size_t)n8 * 128; \
      _Pragma("unroll 1") for (int j = 0; j < 23; ++j) { \
        DMA1S(j < 16 ? sb + (size_t)j * 8192 : sd + (size_t)(j - 16) * 8192, voff, lb + L_A + j * 1024); \
        if (last) { if (j == 7) { VMWAIT(8); CT[C_LANDED] = 2u + 2u * NTILE; } } \
        else if ((j & 7) == 7) VMWAIT(16); \
      } } while (0)
    unsigned seen = 0;
    unsigned pb = (unsigned)__builtin_amdgcn_s_memrealtime() + (unsigned)(PACE_A - PACE_D);
    int pr = PACE_R;
    CT[C_P0] = pb;
    bool deep = true;
#pragma unroll 1
    for (int n = 0; n < NTILE; ++n) {
      const int tile = ((n + 4 * b) & 31) * 4 + s;
      const int cs = n & 3;
      if (n == WO_AT) LOAD_WO(false);
      if (n == WO_AT + 2) CT[C_WOK] = 1u;
#pragma unroll
      for (int hv = 0; hv < 2; ++hv) {
        const unsigned f = 2u + 2u * n + hv;
        if (n >= 4 || (n == 3 && hv == 1)) {
          volatile LAS unsigned* fw = n >= 4 ? (CT + C_DONE + cs) : (CT + C_GUFREE);
          const unsigned need = n >= 4 ? (unsigned)(2 * (n >> 2) - 1 + hv) : 1u;
          if (n < 4 || seen < need) {
            if (*fw < need) {
              VMWAIT(0); CT[C_LANDED] = f;
              lds_wait_ge(fw, need);
            }
          }
        }
        const unsigned char* sb = (hv ? vb : kb) + (size_t)tile * SLOT;
        const unsigned dst = lb + L_RING + (hv * 4 + cs) * SLOT;
        if (hv == 0) { DMA4(sb, dst, ko[0], ko[1], ko[2], ko[3], KPOL); DMA4(sb + 4096, dst + 4096, ko[0], ko[1], ko[2], ko[3], KPOL); }
        else         { DMA4(sb, dst, vo[0], vo[1], vo[0], vo[1], VPOL); DMA4(sb + 4096, dst + 4096, vo[0], vo[1], vo[0], vo[1], VPOL); }
        if (deep) VMWAIT(32); else VMWAIT(16);
        if (hv == 0) { DMA4(sb + 8192, dst + 8192, ko[0], ko[1], ko[2], ko[3], KPOL); DMA4(sb + 12288, dst + 12288, ko[0], ko[1], ko[2], ko[3], KPOL); }
        else         { DMA4(sb + 8192, dst + 8192, vo[0], vo[1], vo[0], vo[1], VPOL); DMA4(sb + 12288, dst + 12288, vo[0], vo[1], vo[0], vo[1], VPOL); }
        const unsigned nx = CT[C_DONE + (hv ? ((cs + 1) & 3) : cs)];
        const unsigned now = (unsigned)__builtin_amdgcn_s_memrealtime();
        const v2u ps = *(volatile LAS v2u*)(CT + C_PB);
        if (deep) { VMWAIT(32); CT[C_LANDED] = f - 1; } else { VMWAIT(16); CT[C_LANDED] = f; }
        CT[C_LAG] = now - (unsigned)(PACE_R * (int)f / 10);
        if (__builtin_amdgcn_readfirstlane(ps.y)) { pb = __builtin_amdgcn_readfirstlane(ps.x); pr = __builtin_amdgcn_readfirstlane(ps.y); }
        deep = (int)(now - pb - (unsigned)(pr * (int)f / 10 + (n >= WO_AT ? 80 : 0))) > PACE_D && f < 2u * NTILE;
        seen = __builtin_amdgcn_readfirstlane(nx);
        if (f == 12) TS(1);
        if (f == 3) TS(5);
        if (f == 6) TS(6);
        if (f == 34) TS(7);
      }
    }
    CT[C_TGO] = pb + (unsigned)(pr * (2 * NTILE + 1) / 10 + H1_GO);
    {
      if (WO_AT < NTILE) {
        VMWAIT(16); CT[C_LANDED] = 1u + 2u * NTILE;
        VMWAIT(0); CT[C_LANDED] = 2u + 2u * NTILE;
        TS(2);
      } else {
        VMWAIT(16); CT[C_LANDED] = 1u + 2u * NTILE;
        LOAD_WO(true);
        TS(2);
        VMWAIT(0);
      }
      CT[C_WOK] = 1u;
      TS(3);
      TS_FLUSH(0);
    }
    return;
  }

  const int c = wave - 1;
  u64 T64 = 0;
  v4u xf[8];
  if (wave == 5) T64 = __hip_atomic_load(ws64 + W_EPOCH / 8, RLX_AGENT);
  if (wave <= 2) {
    const GAS v4u* xp = (const GAS v4u*)(a.x + (size_t)(r & 7) * 1024 + (wave == 1 ? qk * 512 : fk * 256) + g * 16);
#pragma unroll
    for (int ks = 0; ks < 8; ++ks) xf[ks] = xp[(wave == 1 ? ks : (ks & 3)) * 4];
  }
  if (wave == 3) ((LAS unsigned*)(L + L_CTRL))[lane] = 0u;
  if (wave == 4) ((LAS v4u*)(L + L_B + 7168))[lane] = v4u{0u, 0u, 0u, 0u};
  LDS_WAIT();
  __builtin_amdgcn_s_barrier();
  CBAR();

  if (wave == 5) {
    T64 += EPOCH_STEP;
    if (lane == 0) { CT[C_TAG] = (unsigned)T64; CT[C_TAG + 1] = (unsigned)(T64 >> 32); LDS_WAIT(); CT[C_TAGOK] = 1u; }
#define PACE_POST(k) do { if (lane == 0) __hip_atomic_store(ws64 + (W_LAG + (k) * 2048) / 8 + bid, (T64 << 32) | CT[C_LAG], RLX_AGENT); } while (0)
#define PACE_MEAN(k, m, ok) do { const unsigned p0 = CT[C_P0]; \
      const u64 lv = __hip_atomic_load(ws64 + (W_LAG + (k) * 2048) / 8 + kap * 32 + (lane & 31), RLX_AGENT); \
      const bool vk = (unsigned)(lv >> 32) == (unsigned)T64 && lane < 32; \
      int dl = vk ? (int)((unsigned)lv - p0) : 0; dl = dl > 2000 ? 2000 : dl < -2000 ? -2000 : dl; \
      float sm = (float)dl; sm += dpp<0xB1>(sm); sm += dpp<0x4E>(sm); sm += dpp<0x141>(sm); sm += dpp<0x140>(sm); sm = xrow_sum(sm); \
      const int cn = __builtin_popcountll(__ballot(vk)); ok = cn >= 24; m = sm / (float)(cn > 0 ? cn : 1); } while (0)
#define PACE_SET(m, f, dr) do { int nr = PACE_R + (int)(dr); nr = nr < 520 ? 520 : nr > 680 ? 680 : nr; \
      if (lane == 0) *(volatile LAS v2u*)(CT + C_PB) = v2u{CT[C_P0] + (unsigned)(int)(m) - (unsigned)((nr - PACE_R) * (f) / 10), (unsigned)nr}; } while (0)
    float m0; bool ok0;
    if (bid == NWG - 1) {
      const int gb = lane >> 3, part = lane & 7;
      const GAS v4u* xp = (const GAS v4u*)(a.x + (size_t)gb * 1024 + part * 128);
      const GAS v4u* gp = (const GAS v4u*)(a.gw + part * 128);
      float ga = 0.f;
#pragma unroll
      for (int i = 0; i < 8; ++i) {
        const v4u xv = xp[i], gv = gp[i];
#pragma unroll
        for (int e = 0; e < 4; ++e) { ga = fmaf(bflo(xv[e]), bflo(gv[e]), ga); ga = fmaf(bfhi(xv[e]), bfhi(gv[e]), ga); }
      }
      ga += dpp<0xB1>(ga); ga += dpp<0x4E>(ga); ga += dpp<0x141>(ga);
      const float gate = 1.f / (1.f + __expf(-ga));
      if (part == 0) __hip_atomic_store(ws64 + W_G / 8 + gb, ((u64)TAG_HI << 32) | (f24(gate) | TAG_LO), RLX_AGENT);
    }
    {
      lds_wait_ge(CT + C_LANDED, QG_AT);
      const unsigned qo = (unsigned)W_QG + (unsigned)((b * HKV + kap) * 8192 + lane * 16);
      _Pragma("unroll 1") for (unsigned sp = 0; sp < GSPIN; ++sp) {
        v4u v[8]; bool ok = true;
#pragma unroll
        for (int i = 0; i < 8; ++i) v[i] = ld16(rs, qo + i * 1024u);
#pragma unroll
        for (int i = 0; i < 4; ++i) {
          ok &= TAG_OK(v[i].x, v[i].y) && TAG_OK(v[i].z, v[i].w) && TAG_OK(v[i + 4].x, v[i + 4].y) && TAG_OK(v[i + 4].z, v[i + 4].w);
          ((LAS unsigned*)(L + L_Q))[i * 64 + lane] = cvtpk(__uint_as_float(v[i].x << 8) + __uint_as_float(v[i + 4].x << 8), __uint_as_float(v[i].z << 8) + __uint_as_float(v[i + 4].z << 8));
        }
        if (__all(ok)) break;
        __builtin_amdgcn_s_sleep(8);
      }
      LDS_WAIT(); CT[C_QRDY] = 1u;
      TS(2);
    }
    {
      lds_wait_ge(CT + C_LANDED, 12u);
      PACE_POST(0);
      const int fb = lane < 32 ? kap * 32 + lane : (bid & 7) + 8 * ((fi & ~3) + (lane & 3));
      _Pragma("unroll 1") for (unsigned sp = 0; sp < GSPIN; ++sp) {
        if (__all(__hip_atomic_load(ws64 + W_FLAG / 8 + fb, RLX_AGENT) == T64)) break;
        __builtin_amdgcn_s_sleep(32);
      }
      CBAR();
    }
    if (has_ffn) {
      const unsigned go = (unsigned)W_GP + (unsigned)((fcg * 4 + fk) * 2048 + lane * 16);
      const f32x4 p0 = __builtin_bit_cast(f32x4, ld16(rs, go)), p1 = __builtin_bit_cast(f32x4, ld16(rs, go + 1024u));
      unsigned hw[4];
#pragma unroll
      for (int e = 0; e < 4; ++e) {
        const unsigned t = __float_as_uint(p0[e] + p1[e]);
        auto s32 = __builtin_amdgcn_permlane32_swap(t, t, false, false);
        const unsigned tot = __float_as_uint(__uint_as_float(s32[0]) + __uint_as_float(s32[1]));
        auto s16 = __builtin_amdgcn_permlane16_swap(tot, tot, false, false);
        const float gt = __uint_as_float(s16[0]), up = __uint_as_float(s16[1]);
        const float u = 0.7978845608028654f * (gt + 0.044715f * gt * gt * gt);
        const float th = 1.f - 2.f / (__expf(2.f * u) + 1.f);
        hw[e] = f24(0.5f * gt * (1.f + th) * up) | TAG_LO;
      }
      const int src = ((2 * (g & 1) + (r >> 3)) * 4 + ((r & 7) >> 1)) * 4;
      const unsigned a0 = __builtin_amdgcn_ds_bpermute(src, (int)hw[0]), a1 = __builtin_amdgcn_ds_bpermute(src, (int)hw[1]);
      const unsigned a2 = __builtin_amdgcn_ds_bpermute(src, (int)hw[2]), a3 = __builtin_amdgcn_ds_bpermute(src, (int)hw[3]);
      if (lane < 32)
        st16(rs, (unsigned)W_HG + (unsigned)((4 * (fk & 1) + 2 * g + (r >> 3)) * FF + fcg * 32 + (fk >> 1) * 16 + 2 * (r & 7)) * 8u,
             v4u{(r & 1) ? a2 : a0, TAG_HI, (r & 1) ? a3 : a1, TAG_HI});
    }
    {
      lds_wait_ge(CT + C_LANDED, 20u);
      PACE_MEAN(0, m0, ok0);
      if (ok0) PACE_SET(m0, 12, m0 * PACE_G0);
      const bool pad = (lane & 31) >= 28;
      const unsigned ho = (unsigned)W_HG + (unsigned)((lane >> 5) * FF + ht * 56 + (pad ? 0 : 2 * (lane & 31))) * 8u;
      _Pragma("unroll 1") for (unsigned sp = 0; sp < GSPIN; ++sp) {
        v4u v[4];
#pragma unroll
        for (int i = 0; i < 4; ++i) v[i] = ld16(rs, ho + (unsigned)(i * 2 * FF * 8));
        const u64 gv = __hip_atomic_load(ws64 + W_G / 8 + (lane & 7), RLX_AGENT);
        bool ok = TAG_OK((unsigned)gv, (unsigned)(gv >> 32));
#pragma unroll
        for (int i = 0; i < 4; ++i) {
          ok &= TAG_OK(v[i].x, v[i].y) && TAG_OK(v[i].z, v[i].w);
          *(LAS unsigned*)(L + L_HB + (2 * i + (lane >> 5)) * HB_ROW + (lane & 31) * 4) =
              pad ? 0u : cvtpk(__uint_as_float(v[i].x << 8), __uint_as_float(v[i].z << 8));
        }
        if (lane < 8) CT[C_G + lane] = (unsigned)gv << 8;
        if (__all(ok)) break;
        __builtin_amdgcn_s_sleep(32);
      }
      LDS_WAIT();
      CT[C_CLEAN] = 1u;
      TS(1);
    }
    {
      float m1, m2; bool ok1, ok2;
      lds_wait_ge(CT + C_LANDED, 27u);
      PACE_POST(1);
      lds_wait_ge(CT + C_LANDED, 34u);
      PACE_MEAN(1, m1, ok1);
      if (ok0 && ok1) PACE_SET(m1, 27, (m1 - m0) * (10.f / 15.f));
      lds_wait_ge(CT + C_LANDED, 43u);
      PACE_POST(2);
      lds_wait_ge(CT + C_LANDED, 50u);
      PACE_MEAN(2, m2, ok2);
      if (ok1 && ok2) PACE_SET(m2, 43, (m2 - m1) * (10.f / 16.f));
    }
    TS(3);
    {
      const unsigned oo = (unsigned)W_OG + (unsigned)(bid * 4096), ol = (unsigned)lane * 16u;
      float t0[4], t1[4];
      v4u va[4], vb[4];
      unsigned need = 15u, ca, cb;
#define H2_ISSUE(v, cm) do { cm = need; _Pragma("unroll") for (int i = 0; i < 4; ++i) v[i] = ld16(rs, ((cm >> i) & 1u) ? oo + i * 1024u + ol : (unsigned)W_QG + (unsigned)bid * 2048u); } while (0)
#define H2_CHECK(v, cm) do { _Pragma("unroll") for (int i = 0; i < 4; ++i) if (((cm & need) >> i) & 1u) { \
        if (__all(TAG_OK(v[i].x, v[i].y) && TAG_OK(v[i].z, v[i].w))) { t0[i] = __uint_as_float(v[i].x << 8); t1[i] = __uint_as_float(v[i].z << 8); need &= ~(1u << i); } } } while (0)
#pragma unroll 1
      for (unsigned sp = 0; sp < LSPIN; ++sp) { const v4u fl = *(volatile LAS v4u*)(CT + C_STG); if (fl.x & fl.y & fl.z & fl.w) break; __builtin_amdgcn_s_sleep(1); }
      H2_ISSUE(va, ca);
      _Pragma("unroll 1") for (unsigned sp = 0; sp < GSPIN; ++sp) {
        __builtin_amdgcn_s_sleep(H2_SLEEP);
        H2_ISSUE(vb, cb);
        H2_CHECK(va, ca);
        if (need == 0u) break;
        __builtin_amdgcn_s_sleep(H2_SLEEP);
        H2_ISSUE(va, ca);
        H2_CHECK(vb, cb);
        if (need == 0u) break;
      }
      float s0 = (t0[0] + t0[1]) + (t0[2] + t0[3]), s1 = (t1[0] + t1[1]) + (t1[2] + t1[3]);
      s0 += dpp<0x128>(s0); s1 += dpp<0x128>(s1);
      s0 = xrow_sum(s0); s1 = xrow_sum(s1);
      if (lane < 8) ((GAS unsigned*)a.out)[bid * 8 + lane] = cvtpk(s0, s1);
      TS(4);
      if (bid == 0 && lane == 0) __hip_atomic_store(ws64 + W_EPOCH / 8, T64, RLX_AGENT);
    }
    TS_FLUSH(5);
    return;
  }

  if (c == 0) {
    lds_wait_ge(CT + C_TAGOK, 1u);
    T64 = ((u64)CT[C_TAG + 1] << 32) | CT[C_TAG];
    lds_wait_ge(CT + C_LANDED, 1u);
    TS(0);
    LAS unsigned char* sl = L + L_A + (8 * g + (r >> 2)) * 64 + (r & 3) * 8;
    f32x4 d[2] = {{0.f, 0.f, 0.f, 0.f}, {0.f, 0.f, 0.f, 0.f}};
#pragma unroll
    for (int ks = 0; ks < 8; ++ks)
#pragma unroll
      for (int ct = 0; ct < 2; ++ct) {
        const v2u b0 = trread(sl + ks * 2048 + ct * 32), b1 = trread(sl + ks * 2048 + ct * 32 + 256);
        d[ct] = mfma(xf[ks], v4u{b0.x, b0.y, b1.x, b1.y}, d[ct]);
      }
    const f32x4 dv = g < 2 ? d[0] : d[1];
    gu64* qg = ws64 + W_QG / 8 + (size_t)((4 * (g & 1) * HKV + kap) * 2 + qk) * 512 + (qcol & 511) + (g >> 1) * 16 + r;
#pragma unroll
    for (int e = 0; e < 4; ++e) __hip_atomic_store(qg + (size_t)e * HKV * 1024, ((u64)TAG_HI << 32) | (f24(dv[e]) | TAG_LO), RLX_AGENT);
    TS(1);
  } else if (c == 1) {
    lds_wait_ge(CT + C_TAGOK, 1u);
    T64 = ((u64)CT[C_TAG + 1] << 32) | CT[C_TAG];
    const v4u inv = {0u, ~(unsigned)T64, 0u, ~(unsigned)T64};
    const unsigned co = (unsigned)W_PG + (unsigned)(kap * 4 + (lane >> 5)) * PGB + (unsigned)(jg * 512 + (lane & 31) * 16);
    st16(rs, co, inv);
    st16(rs, co + 2u * PGB, inv);
    lds_wait_ge(CT + C_LANDED, 2u);
    LAS unsigned char* sl = L + L_RING + 7 * SLOT + g * 1024 + (r >> 2) * 64 + (r & 3) * 8;
    f32x4 d[4] = {{0.f, 0.f, 0.f, 0.f}, {0.f, 0.f, 0.f, 0.f}, {0.f, 0.f, 0.f, 0.f}, {0.f, 0.f, 0.f, 0.f}};
#pragma unroll
    for (int ks = 0; ks < 4; ++ks)
#pragma unroll
      for (int mc = 0; mc < 4; ++mc) {
        LAS unsigned char* q = sl + ks * 4096 + (mc >> 1) * 512 + (mc & 1) * 32;
        const v2u b0 = trread(q), b1 = trread(q + 256);
        d[mc] = mfma(xf[ks], v4u{b0.x, b0.y, b1.x, b1.y}, d[mc]);
      }
    LDS_WAIT();
    CT[C_GUFREE] = 1u;
    if (has_ffn) {
      const f32x4 dg = g < 2 ? d[0] : d[1], du = g < 2 ? d[2] : d[3];
      GAS unsigned* gp = (GAS unsigned*)(a.ws + W_GP + (size_t)(fcg * 4 + 2 * (g >> 1) + (g & 1)) * 2048 + fk * 512) + r;
#pragma unroll
      for (int e = 0; e < 4; ++e) {
        __hip_atomic_store(gp + e * 16, __float_as_uint(dg[e]), RLX_AGENT);
        __hip_atomic_store(gp + 64 + e * 16, __float_as_uint(du[e]), RLX_AGENT);
      }
    }
    VMWAIT(0);
    if (lane == 0) __hip_atomic_store(ws64 + W_FLAG / 8 + bid, T64, RLX_AGENT);
  } else {
    lds_wait_ge(CT + C_TAGOK, 1u);
    T64 = ((u64)CT[C_TAG + 1] << 32) | CT[C_TAG];
  }
  const unsigned T32 = (unsigned)T64;
  const v4u ones = {0x3F803F80u, 0x3F803F80u, 0x3F803F80u, 0x3F803F80u};
  LAS unsigned char* kbase = L + L_RING + c * SLOT + r * 256;
  LAS unsigned char* vbase = L + L_RING + (4 + c) * SLOT + (4 * g + (r >> 2)) * 256 + (r & 3) * 8;
  const int vx = 4 * (g & 1) + (r >> 2);

  v4u qf[4];
  f32x4 acc[8], accl = {0.f, 0.f, 0.f, 0.f};
#pragma unroll
  for (int m = 0; m < 8; ++m) acc[m] = f32x4{0.f, 0.f, 0.f, 0.f};
  float mrun = -INFINITY, fown = 0.f;
  v4u mlw = {0u, 0u, 0u, 0u};
  f32x4 basea = {0.f, 0.f, 0.f, 0.f}, baseb = {0.f, 0.f, 0.f, 0.f};
#pragma unroll 1
  for (int lap = 0; lap < NTILE / 4; ++lap) {
    const unsigned n = 4u * lap + c;
    lds_wait_ge(CT + C_LANDED, 3u + 2u * n);
    v4u kf[4][4];
#pragma unroll
    for (int T = 0; T < 4; ++T)
#pragma unroll
      for (int ks = 0; ks < 4; ++ks) kf[T][ks] = *(const LAS v4u*)(kbase + T * 4096 + (((4 * ks + g) ^ r) * 16));
    LDS_WAIT();
    CT[C_DONE + c] = (unsigned)(2 * lap + 1);
#pragma unroll
    for (int T = 0; T < 4; ++T) asm volatile("" : "+v"(kf[T][0]), "+v"(kf[T][1]), "+v"(kf[T][2]), "+v"(kf[T][3]) :: "memory");
    v4u vf[2][8];
#define READ_V() do { \
      lds_wait_ge(CT + C_LANDED, 4u + 2u * n); \
      _Pragma("unroll") for (int ks = 0; ks < 2; ++ks) \
        _Pragma("unroll") for (int m = 0; m < 8; ++m) { \
          LAS unsigned char* p = vbase + ks * 8192 + ((m ^ vx) * 32); \
          const v2u t0 = trread(p), t1 = trread(p + 4096); \
          vf[ks][m] = v4u{t0.x, t0.y, t1.x, t1.y}; \
        } \
      LDS_WAIT(); \
      CT[C_DONE + c] = (unsigned)(2 * lap + 2); \
      _Pragma("unroll") for (int ks = 0; ks < 2; ++ks) { \
        asm volatile("" : "+v"(vf[ks][0]), "+v"(vf[ks][1]), "+v"(vf[ks][2]), "+v"(vf[ks][3]) :: "memory"); \
        asm volatile("" : "+v"(vf[ks][4]), "+v"(vf[ks][5]), "+v"(vf[ks][6]), "+v"(vf[ks][7]) :: "memory"); \
      } } while (0)
    if (lap == 0) {
      READ_V();
      lds_wait_ge(CT + C_QRDY, 1u);
#pragma unroll
      for (int ks = 0; ks < 4; ++ks) qf[ks] = *(const LAS v4u*)(L + L_Q + ((r >> 2) * 128 + 32 * ks + 8 * g) * 2);
    }
    f32x4 sc[4];
#pragma unroll
    for (int T = 0; T < 4; ++T) {
      f32x4 d = {0.f, 0.f, 0.f, 0.f};
#pragma unroll
      for (int ks = 0; ks < 4; ++ks) d = mfma(kf[T][ks], qf[ks], d);
      sc[T] = d;
    }
    float tm = fmaxf(fmaxf(sc[0][0], sc[0][1]), fmaxf(sc[0][2], sc[0][3]));
#pragma unroll
    for (int T = 1; T < 4; ++T) tm = fmaxf(tm, fmaxf(fmaxf(sc[T][0], sc[T][1]), fmaxf(sc[T][2], sc[T][3])));
    tm = xrow_max(tm);
    if (__any(tm > mrun)) {
      const float mn = fmaxf(mrun, tm);
      const float al = __builtin_amdgcn_exp2f((mrun - mn) * SC2);
#pragma unroll
      for (int m = 0; m < 8; ++m) acc[m] *= al;
      accl *= al;
      mrun = mn;
    }
    const float mo = -mrun * SC2;
    v4u pf[2];
#pragma unroll
    for (int T = 0; T < 4; ++T) {
      float p[4];
#pragma unroll
      for (int e = 0; e < 4; ++e) p[e] = __builtin_amdgcn_exp2f(fmaf(sc[T][e], SC2, mo));
      pf[T >> 1][(T & 1) * 2] = cvtpk(p[0], p[1]);
      pf[T >> 1][(T & 1) * 2 + 1] = cvtpk(p[2], p[3]);
    }
    accl = mfma(ones, pf[0], accl);
    accl = mfma(ones, pf[1], accl);
    asm volatile("" : "+v"(pf[0]), "+v"(pf[1]), "+v"(accl) :: "memory");
    if (n == NTILE - 1) {
      float mw[3], lw[3]; f32x4 oa[3], ob[3];
#pragma unroll
      for (int w = 0; w < 3; ++w) {
        lds_wait_ge(CT + C_MRG + w, 1u);
        const LAS float* rw = (const LAS float*)(L + L_RING + w * SLOT);
        mw[w] = rw[512 + (r >> 2)]; lw[w] = rw[516 + (r >> 2)];
        oa[w] = *(const LAS f32x4*)(rw + r * 32 + 4 * g); ob[w] = *(const LAS f32x4*)(rw + r * 32 + 16 + 4 * g);
      }
      const unsigned m24 = f24(fmaxf(fmaxf(mrun, mw[0]), fmaxf(mw[1], mw[2])) * SC2);
      const float Mr = __uint_as_float(m24 << 8);
      fown = __builtin_amdgcn_exp2f(fmaf(mrun, SC2, -Mr));
      float Ls = fown * accl[0];
#pragma unroll
      for (int w = 0; w < 3; ++w) {
        const float f = __builtin_amdgcn_exp2f(fmaf(mw[w], SC2, -Mr));
        Ls = fmaf(f, lw[w], Ls); basea += f * oa[w]; baseb += f * ob[w];
      }
      const float inv = 1.f / Ls;
      fown *= inv; basea *= inv; baseb *= inv;
      mlw = v4u{m24 | TAG_LO, TAG_HI, f24(Ls) | TAG_LO, TAG_HI};
    }
    if (lap) READ_V();
#pragma unroll
    for (int ks = 0; ks < 2; ++ks)
#pragma unroll
      for (int m = 0; m < 8; ++m) acc[m] = mfma(vf[ks][m], pf[ks], acc[m]);
  }
  TS(4);
  if (c == 3) {
    const int u = r & 3;
    const f32x4 sa = u == 0 ? acc[0] : u == 1 ? acc[2] : u == 2 ? acc[4] : acc[6], sb = u == 0 ? acc[1] : u == 1 ? acc[3] : u == 2 ? acc[5] : acc[7];
    const f32x4 xa = fown * sa + basea, xb = fown * sb + baseb;
    const unsigned po = (unsigned)W_PG + (unsigned)(kap * 4 + (r >> 2)) * PGB + (unsigned)(jg * 512 + g * 64 + u * 16);
    const v2u pa = {cvtpk(xa[0], xa[1]), cvtpk(xa[2], xa[3])}, pb = {cvtpk(xb[0], xb[1]), cvtpk(xb[2], xb[3])};
    st16(rs, po, v4u{pa.x, T32, pa.y, T32});
    st16(rs, po + 256u, v4u{pb.x, T32, pb.y, T32});
    if ((lane & 51) == 0) st16(rs, (unsigned)W_PG + (unsigned)(kap * 4 + (r >> 2)) * PGB + (unsigned)(16384 + jg * 128), mlw);
    if ((r >> 2) == hh) {
      LAS unsigned char* own = L + L_RING + (jg >> 3) * SLOT + 4096 + (jg & 7) * STG_ROW + 64 * u + 8 * g;
      *(LAS v2u*)own = pa; *(LAS v2u*)(own + 32) = pb;
      if ((lane & 51) == 0) *(LAS v2u*)(L + L_ML + jg * 8) = v2u{mlw.x << 8, mlw.z << 8};
    }
    CT[C_SEEN] = 1u;
    TS(2);
  } else {
    LAS float* rec = (LAS float*)(L + L_RING + c * SLOT);
    if ((r & 3) == 0) {
#pragma unroll
      for (int m = 0; m < 8; ++m) *(LAS f32x4*)(rec + (r >> 2) * 128 + 16 * m + 4 * g) = acc[m];
      if (g == 0) { rec[512 + (r >> 2)] = mrun; rec[516 + (r >> 2)] = accl[0]; }
    }
    LDS_WAIT();
    CT[C_MRG + c] = 1u;
  }
  lds_wait_ge(CT + C_CLEAN, 1u);
  {
    LAS unsigned* stw = (LAS unsigned*)(L + L_RING + c * SLOT + 4096) + 16 * ((lane >> 1) & 3) + 8 * (lane >> 5) + 2 * ((lane >> 3) & 3) + (lane & 1);
    gu64* pgp = (gu64*)(a.ws + W_PG + (size_t)ht * PGB + c * 4096);
    gu64* dmy = ws64 + W_QG / 8 + (size_t)bid * 256;
    gu64* mlp = (gu64*)(a.ws + W_PG + (size_t)ht * PGB + 16384 + (8 * c + ((lane >> 1) & 7)) * 128) + (lane & 1);
    unsigned need = c == (jg >> 3) ? 0x1ffu & ~(1u << (jg & 7)) : 0x1ffu, ca = 0u, cb = 0u;
    u64 va[9], vb[9];
#define H1_ISSUE(v, cm) do { cm = need; \
      _Pragma("unroll") for (int i = 0; i < 8; ++i) v[i] = __hip_atomic_load(((cm >> i) & 1u) ? pgp + i * 64 + lane : dmy, RLX_AGENT); \
      v[8] = __hip_atomic_load(!mlok ? mlp : dmy, RLX_AGENT); } while (0)
#define H1_CHECK(v, cm) do { _Pragma("unroll") for (int i = 0; i < 8; ++i) \
      if (((cm & need) >> i) & 1u) { if (__all((unsigned)(v[i] >> 32) == T32)) { stw[i * (STG_ROW / 4)] = (unsigned)v[i]; need &= ~(1u << i); } } \
      if (!mlok && TAG_OK((unsigned)v[8], (unsigned)(v[8] >> 32))) { ((LAS unsigned*)(L + L_ML))[16 * c + lane] = (unsigned)v[8] << 8; mlok = true; } \
      if (__all(mlok)) need &= 0xffu; } while (0)
    bool mlok = lane >= 16 || (c == (jg >> 3) && (lane >> 1) == (jg & 7));
    v4u bw[4]; f32x4 dff = {0.f, 0.f, 0.f, 0.f};
    bool pre = false;
#define PRE_WORK() do { \
      LAS unsigned char* wb = L + L_A + (8 * g + (r >> 2)) * 128 + c * 32 + (r & 3) * 8; \
      _Pragma("unroll") for (int ks = 0; ks < 4; ++ks) { const v2u t0 = trread(wb + ks * 4096), t1 = trread(wb + ks * 4096 + 512); bw[ks] = v4u{t0.x, t0.y, t1.x, t1.y}; } \
      _Pragma("unroll") for (int ks = 0; ks < 2; ++ks) { \
        const v2u t0 = trread(wb + SLOT + ks * 4096), t1 = trread(wb + SLOT + ks * 4096 + 512); \
        const v4u hf = *(const LAS v4u*)(L + L_HB + (4 * (r & 1) + (r >> 2)) * HB_ROW + 64 * ks + 16 * g); \
        dff = mfma(hf, v4u{t0.x, t0.y, t1.x, t1.y}, dff); \
      } \
      asm volatile("" : "+v"(bw[0]), "+v"(bw[1]), "+v"(bw[2]), "+v"(bw[3]), "+v"(dff) :: "memory"); } while (0)
    lds_wait_ge(CT + C_SEEN, 1u);
    {
      const unsigned tgo = CT[C_TGO];
#pragma unroll 1
      for (int sp = 0; sp < 256 && (int)((unsigned)__builtin_amdgcn_s_memrealtime() - tgo) < 0; ++sp) __builtin_amdgcn_s_sleep(1);
    }
    H1_ISSUE(va, ca);
    _Pragma("unroll 1") for (unsigned sp = 0; sp < GSPIN; ++sp) {
      if (__builtin_popcount(need) <= H1_DIRECT) __builtin_amdgcn_s_sleep(H1_SLEEP); else __builtin_amdgcn_s_sleep(ML_SLEEP);
      H1_ISSUE(vb, cb);
      if (!pre && CT[C_WOK] != 0u) { CBAR(); PRE_WORK(); pre = true; }
      H1_CHECK(va, ca);
      if (need == 0u) break;
      if (__builtin_popcount(need) <= H1_DIRECT) __builtin_amdgcn_s_sleep(H1_SLEEP); else __builtin_amdgcn_s_sleep(ML_SLEEP);
      H1_ISSUE(va, ca);
      H1_CHECK(vb, cb);
      if (need == 0u) break;
    }
    TS(6);
    LDS_WAIT();
    CT[C_STG + c] = 1u;
    if (!pre) { lds_wait_ge(CT + C_WOK, 1u); PRE_WORK(); }
    TS(5);
#pragma unroll 1
    for (unsigned sp = 0; sp < LSPIN; ++sp) { const v4u fl = *(volatile LAS v4u*)(CT + C_STG); if (fl.x & fl.y & fl.z & fl.w) break; }
    CBAR();
    if (c == 3) TS(3);
    const LAS float* mls = (const LAS float*)(L + L_ML);
    LAS unsigned char* stg = L + L_RING + (r >> 3) * SLOT + 4096 + (r & 7) * STG_ROW + 16 * g;
    unsigned y[2];
#pragma unroll
    for (int rt = 0; rt < 2; ++rt) {
      f32x4 d = {0.f, 0.f, 0.f, 0.f};
#pragma unroll
      for (int ks = 0; ks < 4; ++ks) d = mfma(*(const LAS v4u*)(stg + 2 * rt * SLOT + 64 * ks), bw[ks], d);
      const f32x4 m01 = *(const LAS f32x4*)(mls + 32 * rt + 8 * g), m23 = *(const LAS f32x4*)(mls + 32 * rt + 8 * g + 4);
      const float Mx = fmaxf(fmaxf(m01[0], m01[2]), fmaxf(m23[0], m23[2]));
      const float w0 = __builtin_amdgcn_exp2f(m01[0] - Mx) * m01[1], w1 = __builtin_amdgcn_exp2f(m01[2] - Mx) * m01[3];
      const float w2 = __builtin_amdgcn_exp2f(m23[0] - Mx) * m23[1], w3 = __builtin_amdgcn_exp2f(m23[2] - Mx) * m23[3];
      const float gate = __uint_as_float(CT[C_G + 4 * rt + g]);
      const float att = ((w0 * d[0] + w1 * d[1]) + (w2 * d[2] + w3 * d[3])) / ((w0 + w1) + (w2 + w3));
      y[rt] = f24(fmaf(gate, att, (1.f - gate) * dff[rt])) | TAG_LO;
    }
    const unsigned snd = (r & 1) ? y[0] : y[1];
    const unsigned rcv = (unsigned)__builtin_amdgcn_mov_dpp((int)snd, 0xB1, 0xf, 0xf, true);
    st16(rs, (unsigned)W_OG + (unsigned)((((4 * (r & 1) + g) * 32 + 4 * n8 + c) * 32 + ht) * 16 + (r & 14)) * 8u,
         (r & 1) ? v4u{rcv, TAG_HI, y[1], TAG_HI} : v4u{y[0], TAG_HI, rcv, TAG_HI});
  }
  if (c == 0) TS(2);
  TS_FLUSH(wave);
}
}

extern "C" void kernel_launch(void* const* d_in, const int* in_sizes, int n_in, void* d_out, int out_size, void* d_ws,
                              size_t ws_size, hipStream_t stream) {
  (void)in_sizes; (void)n_in; (void)out_size;
  if (ws_size < W_END) return;
  Args a;
  a.x = (const unsigned char*)d_in[0]; a.k = (const unsigned char*)d_in[1]; a.v = (const unsigned char*)d_in[2];
  a.wq = (const unsigned char*)d_in[3]; a.wo = (const unsigned char*)d_in[4];
  a.wg = (const unsigned char*)d_in[6]; a.wu = (const unsigned char*)d_in[7]; a.wd = (const unsigned char*)d_in[8];
  a.gw = (const unsigned char*)d_in[9];
  a.out = (unsigned char*)d_out; a.ws = (unsigned char*)d_ws;
  (void)hipFuncSetAttribute((const void*)fused_kernel, hipFuncAttributeMaxDynamicSharedMemorySize, L_BYTES);
  hipLaunchKernelGGL(fused_kernel, dim3(NWG), dim3(NTHR), L_BYTES, stream, a);
}
```

```cpp
#include <hip/hip_runtime.h>
#include <hip/hip_bf16.h>
#include <stdint.h>

#define GAS __attribute__((address_space(1)))
#define LAS __attribute__((address_space(3)))
typedef unsigned v4u __attribute__((ext_vector_type(4)));
typedef unsigned v2u __attribute__((ext_vector_type(2)));
typedef float f32x4 __attribute__((ext_vector_type(4)));
typedef float f32x2 __attribute__((ext_vector_type(2)));
typedef __bf16 bf16x8 __attribute__((ext_vector_type(8)));
typedef short s16x4 __attribute__((ext_vector_type(4)));
typedef unsigned long long u64;
typedef GAS u64 gu64;
#define RLX_AGENT __ATOMIC_RELAXED, __HIP_MEMORY_SCOPE_AGENT

namespace {
constexpr int NB = 8, DM = 512, HQ = 32, HKV = 8, SK = 8192, DH = 128, FF = 1792;
constexpr int NWG = 256, NTHR = 6 * 64;
constexpr int NTILE = 32;
constexpr int PACE_R = 550, PACE_0 = 46, PACE_D = 44, H1_GO = 170;
constexpr int H1_DIRECT = 4;
constexpr int SLOT = 16384;
constexpr int L_RING = 0, L_A = 8 * SLOT, L_B = L_A + SLOT, L_Q = L_B + 8192, L_HB = L_Q + 1024, L_ML = L_HB + 2048, L_CTRL = L_ML + 256, L_BYTES = L_CTRL + 256;
constexpr int STG_ROW = 272;
constexpr int HB_ROW = 144;
constexpr int C_LANDED = 0, C_GUFREE = 1, C_QRDY = 2, C_PPUB = 3, C_DONE = 4, C_MRG = 8, C_STG = 12, C_TAG = 16, C_TAGOK = 18, C_WOK = 19, C_CLEAN = 20, C_LAG = 21, C_PB = 22, C_PR = 23, C_G = 24, C_P0 = 32, C_TGO = 33;
static_assert(L_BYTES <= 160 * 1024, "LDS");
constexpr size_t W_EPOCH = 0, W_G = 256, W_FLAG = 4096, W_LAG = 8192;
constexpr size_t W_QG = 16384;
constexpr size_t W_GP = W_QG + (size_t)2 * NB * 4096 * 8;
constexpr size_t W_HG = W_GP + (size_t)224 * 2048;
constexpr size_t W_PG = W_HG + (size_t)NB * FF * 8;
constexpr unsigned PGB = 16384 + 4096;
constexpr size_t W_OG = W_PG + (size_t)HQ * PGB;
constexpr size_t W_END = W_OG + (size_t)NWG * 4096;
static_assert(W_END < (1u << 30) && W_PG % 1024 == 0, "buffer offsets are int");
constexpr u64 EPOCH_STEP = 0x9E3779B97F4A7C15ull;
constexpr unsigned GSPIN = 1u << 15, LSPIN = 1u << 18;
constexpr float SC2 = 0.08838834764831845f * 1.4426950408889634f;

struct Args {
  const unsigned char *x, *k, *v, *wq, *wo, *wg, *wu, *wd, *gw;
  unsigned char *out, *ws;
};

__device__ __forceinline__ f32x4 mfma(v4u a, v4u b, f32x4 c) {
  return __builtin_amdgcn_mfma_f32_16x16x32_bf16(__builtin_bit_cast(bf16x8, a), __builtin_bit_cast(bf16x8, b), c, 0, 0, 0);
}
__device__ __forceinline__ v2u trread(LAS unsigned char* p) {
  return __builtin_bit_cast(v2u, __builtin_amdgcn_ds_read_tr16_b64_v4i16((LAS s16x4*)p));
}
__device__ __forceinline__ unsigned cvtpk(float lo, float hi) {
  typedef __bf16 bf16x2 __attribute__((ext_vector_type(2)));
  bf16x2 r; r[0] = (__bf16)lo; r[1] = (__bf16)hi; return __builtin_bit_cast(unsigned, r);
}
template <int CTRL> __device__ __forceinline__ float dpp(float x) {
  return __builtin_bit_cast(float, __builtin_amdgcn_mov_dpp(__builtin_bit_cast(int, x), CTRL, 0xf, 0xf, true));
}
__device__ __forceinline__ float xrow_max(float x) {
  auto s = __builtin_amdgcn_permlane16_swap(__float_as_uint(x), __float_as_uint(x), false, false);
  x = fmaxf(__uint_as_float(s[0]), __uint_as_float(s[1]));
  auto t = __builtin_amdgcn_permlane32_swap(__float_as_uint(x), __float_as_uint(x), false, false);
  return fmaxf(__uint_as_float(t[0]), __uint_as_float(t[1]));
}
__device__ __forceinline__ float xrow_sum(float x) {
  auto s = __builtin_amdgcn_permlane16_swap(__float_as_uint(x), __float_as_uint(x), false, false);
  x = __uint_as_float(s[0]) + __uint_as_float(s[1]);
  auto t = __builtin_amdgcn_permlane32_swap(__float_as_uint(x), __float_as_uint(x), false, false);
  return __uint_as_float(t[0]) + __uint_as_float(t[1]);
}
__device__ __forceinline__ float bflo(unsigned u) { return __uint_as_float(u << 16); }
__device__ __forceinline__ float bfhi(unsigned u) { return __uint_as_float(u & 0xffff0000u); }

__device__ __forceinline__ v4u ld16(__amdgpu_buffer_rsrc_t rs, unsigned off) { return __builtin_amdgcn_raw_buffer_load_b128(rs, (int)off, 0, 16); }
__device__ __forceinline__ void st16(__amdgpu_buffer_rsrc_t rs, unsigned off, v4u v) { __builtin_amdgcn_raw_buffer_store_b128(v, rs, (int)off, 0, 16); }
__device__ __forceinline__ unsigned f24(float v) { return (__float_as_uint(v) + 0x80u) >> 8; }
#define TAG_LO ((unsigned)T64 << 24)
#define TAG_HI ((unsigned)(T64 >> 8))
#define TAG_OK(lo, hi) ((hi) == TAG_HI && ((lo) ^ TAG_LO) < 0x1000000u)
#define CBAR() asm volatile("" ::: "memory")
#define LDS_WAIT() asm volatile("s_waitcnt lgkmcnt(0)" ::: "memory")
#define DMA4(sbase, dst, v0, v1, v2, v3)                                                                         \
  asm volatile("s_mov_b32 m0, %5\n\ts_nop 0\n\t"                                                                 \
               "global_load_lds_dwordx4 %0, %4 nt\n\t"                                                           \
               "global_load_lds_dwordx4 %1, %4 offset:1024 nt\n\t"                                               \
               "global_load_lds_dwordx4 %2, %4 offset:2048 nt\n\t"                                               \
               "global_load_lds_dwordx4 %3, %4 offset:3072 nt"                                                   \
               :: "v"(v0), "v"(v1), "v"(v2), "v"(v3), "s"(sbase), "s"(dst) : "memory")
#define DMA1S(sbase, voff, dst)                                                                                  \
  asm volatile("s_mov_b32 m0, %2\n\ts_nop 0\n\tglobal_load_lds_dwordx4 %0, %1 nt" :: "v"(voff), "s"(sbase), "s"(dst) : "memory")
#define DMA1V(vaddr, dst)                                                                                        \
  asm volatile("s_mov_b32 m0, %1\n\ts_nop 0\n\tglobal_load_lds_dwordx4 %0, off nt" :: "v"(vaddr), "s"(dst) : "memory")
#define VMWAIT(n) asm volatile("s_waitcnt vmcnt(" #n ")" ::: "memory")

__device__ __forceinline__ void lds_wait_ge(volatile LAS unsigned* w, unsigned need) {
#pragma unroll 1
  for (unsigned sp = 0; *w < need && sp < LSPIN; ++sp) __builtin_amdgcn_s_sleep(1);
  CBAR();
}

__global__ void __launch_bounds__(NTHR, 2) fused_kernel(Args a) {
  extern __shared__ __attribute__((aligned(16))) unsigned char smem[];
  LAS unsigned char* L = (LAS unsigned char*)smem;
  volatile LAS unsigned* CT = (volatile LAS unsigned*)(L + L_CTRL);
  const int tid = threadIdx.x, lane = tid & 63, wave = __builtin_amdgcn_readfirstlane(tid >> 6);
  const int bid = blockIdx.x;
  const int kap = bid >> 5, jg = bid & 31, b = jg >> 2, s = jg & 3;
  const int qcol = kap * 512 + (((jg & 7) << 1) | ((jg >> 3) & 1)) * 32, qk = jg >> 4;
  const bool has_ffn = (bid >> 3) < 28;
  const int fi = has_ffn ? (bid >> 3) : 27;
  const int fcg = (bid & 7) * 7 + (fi >> 2), fk = fi & 3;
  const int hh = jg >> 3, ht = kap * 4 + hh, n8 = jg & 7;
  const int g = lane >> 4, r = lane & 15;
  gu64* ws64 = (gu64*)a.ws;
  const __amdgpu_buffer_rsrc_t rs = __builtin_amdgcn_make_buffer_rsrc((void*)a.ws, 0, (int)W_END, 0x00020000);
  if (__builtin_expect(wave == 0, 1)) {
    __builtin_amdgcn_s_waitcnt(0);
    __builtin_amdgcn_s_setprio(3);
    const unsigned lb = (unsigned)(size_t)L;
    {
      const unsigned voff = (unsigned)(lane >> 2) * 8192u + (unsigned)(lane & 3) * 16u;
      const unsigned char* sb = a.wq + (size_t)qk * 256 * 8192 + (size_t)qcol * 2;
#pragma unroll 1
      for (int j = 0; j < 16; ++j) DMA1S(sb + (size_t)j * 16 * 8192, voff, lb + L_A + j * 1024);
    }
    {
      const unsigned char* p = ((lane & 32) ? a.wu : a.wg) + (size_t)(fk * 128 + ((lane >> 2) & 7)) * (FF * 2) + (size_t)fcg * 64 + (lane & 3) * 16;
#pragma unroll 1
      for (int j = 0; j < 16; ++j) DMA1V(p + (size_t)j * 8 * (FF * 2), lb + L_RING + 7 * SLOT + j * 1024);
    }
    __builtin_amdgcn_s_barrier();
    CBAR();
    unsigned ko[4], vo[2];
#pragma unroll
    for (int j = 0; j < 4; ++j) ko[j] = (unsigned)g * 256u + (unsigned)((r ^ g ^ (4 * j)) * 16);
#pragma unroll
    for (int j = 0; j < 2; ++j) vo[j] = (unsigned)g * 256u + (unsigned)((r ^ (2 * (4 * j + g))) * 16);
    const size_t seq_off = (size_t)(b * HKV + kap) * SK * 256;
    const unsigned char* kb = a.k + seq_off;
    const unsigned char* vb = a.v + seq_off;
    unsigned seen = 0;
    unsigned pb = (unsigned)__builtin_amdgcn_s_memrealtime() + (unsigned)PACE_0;
    int pr = PACE_R;
    CT[C_P0] = pb;
    bool deep = true;
#pragma unroll 1
    for (int n = 0; n < NTILE; ++n) {
      const int tile = ((n + 4 * b) & 31) * 4 + s;
      const int cs = n & 3;
#pragma unroll
      for (int hv = 0; hv < 2; ++hv) {
        const unsigned f = 2u + 2u * n + hv;
        if (n >= 4 || (n == 3 && hv == 1)) {
          volatile LAS unsigned* fw = n >= 4 ? (CT + C_DONE + cs) : (CT + C_GUFREE);
          const unsigned need = n >= 4 ? (unsigned)(2 * (n >> 2) - 1 + hv) : 1u;
          if (n < 4 || seen < need) {
            if (*fw < need) {
              VMWAIT(0); CT[C_LANDED] = f;
              lds_wait_ge(fw, need);
            }
          }
        }
        const unsigned char* sb = (hv ? vb : kb) + (size_t)tile * SLOT;
        const unsigned dst = lb + L_RING + (hv * 4 + cs) * SLOT;
        if (hv == 0) { DMA4(sb, dst, ko[0], ko[1], ko[2], ko[3]); DMA4(sb + 4096, dst + 4096, ko[0], ko[1], ko[2], ko[3]); }
        else         { DMA4(sb, dst, vo[0], vo[1], vo[0], vo[1]); DMA4(sb + 4096, dst + 4096, vo[0], vo[1], vo[0], vo[1]); }
        if (deep) VMWAIT(32); else VMWAIT(16);
        if (hv == 0) { DMA4(sb + 8192, dst + 8192, ko[0], ko[1], ko[2], ko[3]); DMA4(sb + 12288, dst + 12288, ko[0], ko[1], ko[2], ko[3]); }
        else         { DMA4(sb + 8192, dst + 8192, vo[0], vo[1], vo[0], vo[1]); DMA4(sb + 12288, dst + 12288, vo[0], vo[1], vo[0], vo[1]); }
        const unsigned nx = CT[C_DONE + (hv ? ((cs + 1) & 3) : cs)];
        const unsigned now = (unsigned)__builtin_amdgcn_s_memrealtime();
        const v2u ps = *(volatile LAS v2u*)(CT + C_PB);
        if (deep) { VMWAIT(32); CT[C_LANDED] = f - 1; } else { VMWAIT(16); CT[C_LANDED] = f; }
        CT[C_LAG] = now - (unsigned)(PACE_R * (int)f / 10);
        if (__builtin_amdgcn_readfirstlane(ps.y)) { pb = __builtin_amdgcn_readfirstlane(ps.x); pr = __builtin_amdgcn_readfirstlane(ps.y); }
        deep = (int)(now - pb - (unsigned)(pr * (int)f / 10)) > PACE_D && f < 2u * NTILE;
        seen = __builtin_amdgcn_readfirstlane(nx);
      }
    }
    CT[C_TGO] = pb + (unsigned)(pr * (2 * NTILE + 1) / 10 + H1_GO);
    VMWAIT(16); CT[C_LANDED] = 1u + 2u * NTILE;
    {
      const unsigned voff = (unsigned)(lane >> 3) * 1024u + (unsigned)(lane & 7) * 16u;
      const unsigned char* sb = a.wo + (size_t)ht * 128 * 1024 + (size_t)n8 * 128;
      const unsigned char* sd = a.wd + (size_t)ht * 56 * 1024 + (size_t)n8 * 128;
#pragma unroll 1
      for (int j = 0; j < 23; ++j) {
        DMA1S(j < 16 ? sb + (size_t)j * 8192 : sd + (size_t)(j - 16) * 8192, voff, lb + L_A + j * 1024);
        if (j == 7) { VMWAIT(8); CT[C_LANDED] = 2u + 2u * NTILE; }
      }
      VMWAIT(0);
      CT[C_WOK] = 1u;
    }
    return;
  }

  const int c = wave - 1;
  u64 T64 = 0;
  v4u xf[8];
  if (wave == 5) T64 = __hip_atomic_load(ws64 + W_EPOCH / 8, RLX_AGENT);
  if (wave <= 2) {
    const GAS v4u* xp = (const GAS v4u*)(a.x + (size_t)(r & 7) * 1024 + (wave == 1 ? qk * 512 : fk * 256) + g * 16);
#pragma unroll
    for (int ks = 0; ks < 8; ++ks) xf[ks] = xp[(wave == 1 ? ks : (ks & 3)) * 4];
  }
  if (wave == 3) ((LAS unsigned*)(L + L_CTRL))[lane] = 0u;
  if (wave == 4) ((LAS v4u*)(L + L_B + 7168))[lane] = v4u{0u, 0u, 0u, 0u};
  LDS_WAIT();
  __builtin_amdgcn_s_barrier();
  CBAR();

  if (wave == 5) {
    T64 += EPOCH_STEP;
    if (lane == 0) { CT[C_TAG] = (unsigned)T64; CT[C_TAG + 1] = (unsigned)(T64 >> 32); LDS_WAIT(); CT[C_TAGOK] = 1u; }
#define PACE_POST(k) do { if (lane == 0) __hip_atomic_store(ws64 + (W_LAG + (k) * 2048) / 8 + bid, (T64 << 32) | CT[C_LAG], RLX_AGENT); } while (0)
#define PACE_MEAN(k, m, ok) do { const unsigned p0 = CT[C_P0]; \
      const u64 lv = __hip_atomic_load(ws64 + (W_LAG + (k) * 2048) / 8 + kap * 32 + (lane & 31), RLX_AGENT); \
      const bool vk = (unsigned)(lv >> 32) == (unsigned)T64 && lane < 32; \
      int dl = vk ? (int)((unsigned)lv - p0) : 0; dl = dl > 2000 ? 2000 : dl < -2000 ? -2000 : dl; \
      float sm = (float)dl; sm += dpp<0xB1>(sm); sm += dpp<0x4E>(sm); sm += dpp<0x141>(sm); sm += dpp<0x140>(sm); sm = xrow_sum(sm); \
      const int cn = __builtin_popcountll(__ballot(vk)); ok = cn >= 24; m = sm / (float)(cn > 0 ? cn : 1); } while (0)
#define PACE_SET(m, f, dr) do { int nr = PACE_R + (int)(dr); nr = nr < 520 ? 520 : nr > 680 ? 680 : nr; \
      if (lane == 0) *(volatile LAS v2u*)(CT + C_PB) = v2u{CT[C_P0] + (unsigned)(int)(m) - (unsigned)((nr - PACE_R) * (f) / 10), (unsigned)nr}; } while (0)
    float m0; bool ok0;
    if (bid == NWG - 1) {
      const int gb = lane >> 3, part = lane & 7;
      const GAS v4u* xp = (const GAS v4u*)(a.x + (size_t)gb * 1024 + part * 128);
      const GAS v4u* gp = (const GAS v4u*)(a.gw + part * 128);
      float ga = 0.f;
#pragma unroll
      for (int i = 0; i < 8; ++i) {
        const v4u xv = xp[i], gv = gp[i];
#pragma unroll
        for (int e = 0; e < 4; ++e) { ga = fmaf(bflo(xv[e]), bflo(gv[e]), ga); ga = fmaf(bfhi(xv[e]), bfhi(gv[e]), ga); }
      }
      ga += dpp<0xB1>(ga); ga += dpp<0x4E>(ga); ga += dpp<0x141>(ga);
      const float gate = 1.f / (1.f + __expf(-ga));
      if (part == 0) __hip_atomic_store(ws64 + W_G / 8 + gb, ((u64)TAG_HI << 32) | (f24(gate) | TAG_LO), RLX_AGENT);
    }
    {
      lds_wait_ge(CT + C_LANDED, 6u);
      const unsigned qo = (unsigned)W_QG + (unsigned)((b * HKV + kap) * 8192 + lane * 16);
      _Pragma("unroll 1") for (unsigned sp = 0; sp < GSPIN; ++sp) {
        v4u v[8]; bool ok = true;
#pragma unroll
        for (int i = 0; i < 8; ++i) v[i] = ld16(rs, qo + i * 1024u);
#pragma unroll
        for (int i = 0; i < 4; ++i) {
          ok &= TAG_OK(v[i].x, v[i].y) && TAG_OK(v[i].z, v[i].w) && TAG_OK(v[i + 4].x, v[i + 4].y) && TAG_OK(v[i + 4].z, v[i + 4].w);
          ((LAS unsigned*)(L + L_Q))[i * 64 + lane] = cvtpk(__uint_as_float(v[i].x << 8) + __uint_as_float(v[i + 4].x << 8), __uint_as_float(v[i].z << 8) + __uint_as_float(v[i + 4].z << 8));
        }
        if (__all(ok)) break;
        __builtin_amdgcn_s_sleep(8);
      }
      LDS_WAIT(); CT[C_QRDY] = 1u;
    }
    {
      lds_wait_ge(CT + C_LANDED, 12u);
      PACE_POST(0);
      const int fb = lane < 32 ? kap * 32 + lane : (bid & 7) + 8 * ((fi & ~3) + (lane & 3));
      _Pragma("unroll 1") for (unsigned sp = 0; sp < GSPIN; ++sp) {
        if (__all(__hip_atomic_load(ws64 + W_FLAG / 8 + fb, RLX_AGENT) == T64)) break;
        __builtin_amdgcn_s_sleep(32);
      }
      CBAR();
    }
    if (has_ffn) {
      const unsigned go = (unsigned)W_GP + (unsigned)((fcg * 4 + fk) * 2048 + lane * 16);
      const f32x4 p0 = __builtin_bit_cast(f32x4, ld16(rs, go)), p1 = __builtin_bit_cast(f32x4, ld16(rs, go + 1024u));
      unsigned hw[4];
#pragma unroll
      for (int e = 0; e < 4; ++e) {
        const unsigned t = __float_as_uint(p0[e] + p1[e]);
        auto s32 = __builtin_amdgcn_permlane32_swap(t, t, false, false);
        const unsigned tot = __float_as_uint(__uint_as_float(s32[0]) + __uint_as_float(s32[1]));
        auto s16 = __builtin_amdgcn_permlane16_swap(tot, tot, false, false);
        const float gt = __uint_as_float(s16[0]), up = __uint_as_float(s16[1]);
        const float u = 0.7978845608028654f * (gt + 0.044715f * gt * gt * gt);
        const float th = 1.f - 2.f / (__expf(2.f * u) + 1.f);
        hw[e] = f24(0.5f * gt * (1.f + th) * up) | TAG_LO;
      }
      const int src = ((2 * (g & 1) + (r >> 3)) * 4 + ((r & 7) >> 1)) * 4;
      const unsigned a0 = __builtin_amdgcn_ds_bpermute(src, (int)hw[0]), a1 = __builtin_amdgcn_ds_bpermute(src, (int)hw[1]);
      const unsigned a2 = __builtin_amdgcn_ds_bpermute(src, (int)hw[2]), a3 = __builtin_amdgcn_ds_bpermute(src, (int)hw[3]);
      if (lane < 32)
        st16(rs, (unsigned)W_HG + (unsigned)((4 * (fk & 1) + 2 * g + (r >> 3)) * FF + fcg * 32 + (fk >> 1) * 16 + 2 * (r & 7)) * 8u,
             v4u{(r & 1) ? a2 : a0, TAG_HI, (r & 1) ? a3 : a1, TAG_HI});
    }
    {
      lds_wait_ge(CT + C_LANDED, 20u);
      PACE_MEAN(0, m0, ok0);
      if (ok0) PACE_SET(m0, 12, m0 * 0.5f);
      const bool pad = (lane & 31) >= 28;
      const unsigned ho = (unsigned)W_HG + (unsigned)((lane >> 5) * FF + ht * 56 + (pad ? 0 : 2 * (lane & 31))) * 8u;
      _Pragma("unroll 1") for (unsigned sp = 0; sp < GSPIN; ++sp) {
        v4u v[4];
#pragma unroll
        for (int i = 0; i < 4; ++i) v[i] = ld16(rs, ho + (unsigned)(i * 2 * FF * 8));
        const u64 gv = __hip_atomic_load(ws64 + W_G / 8 + (lane & 7), RLX_AGENT);
        bool ok = TAG_OK((unsigned)gv, (unsigned)(gv >> 32));
#pragma unroll
        for (int i = 0; i < 4; ++i) {
          ok &= TAG_OK(v[i].x, v[i].y) && TAG_OK(v[i].z, v[i].w);
          *(LAS unsigned*)(L + L_HB + (2 * i + (lane >> 5)) * HB_ROW + (lane & 31) * 4) =
              pad ? 0u : cvtpk(__uint_as_float(v[i].x << 8), __uint_as_float(v[i].z << 8));
        }
        if (lane < 8) CT[C_G + lane] = (unsigned)gv << 8;
        if (__all(ok)) break;
        __builtin_amdgcn_s_sleep(32);
      }
      LDS_WAIT();
      CT[C_CLEAN] = 1u;
    }
    {
      float m1, m2; bool ok1, ok2;
      lds_wait_ge(CT + C_LANDED, 27u);
      PACE_POST(1);
      lds_wait_ge(CT + C_LANDED, 34u);
      PACE_MEAN(1, m1, ok1);
      if (ok0 && ok1) PACE_SET(m1, 27, (m1 - m0) * (10.f / 15.f));
      lds_wait_ge(CT + C_LANDED, 43u);
      PACE_POST(2);
      lds_wait_ge(CT + C_LANDED, 50u);
      PACE_MEAN(2, m2, ok2);
      if (ok1 && ok2) PACE_SET(m2, 43, (m2 - m1) * (10.f / 16.f));
    }
    {
      const unsigned oo = (unsigned)W_OG + (unsigned)(bid * 4096), ol = (unsigned)lane * 16u;
      float t0[4], t1[4];
      v4u va[4], vb[4];
      unsigned need = 15u, ca, cb;
#define H2_ISSUE(v, cm) do { cm = need; _Pragma("unroll") for (int i = 0; i < 4; ++i) v[i] = ld16(rs, ((cm >> i) & 1u) ? oo + i * 1024u + ol : (unsigned)W_QG + (unsigned)bid * 2048u); } while (0)
#define H2_CHECK(v, cm) do { _Pragma("unroll") for (int i = 0; i < 4; ++i) if (((cm & need) >> i) & 1u) { \
        if (__all(TAG_OK(v[i].x, v[i].y) && TAG_OK(v[i].z, v[i].w))) { t0[i] = __uint_as_float(v[i].x << 8); t1[i] = __uint_as_float(v[i].z << 8); need &= ~(1u << i); } } } while (0)
#pragma unroll 1
      for (unsigned sp = 0; sp < LSPIN; ++sp) { const v4u fl = *(volatile LAS v4u*)(CT + C_STG); if (fl.x & fl.y & fl.z & fl.w) break; __builtin_amdgcn_s_sleep(1); }
      H2_ISSUE(va, ca);
      _Pragma("unroll 1") for (unsigned sp = 0; sp < GSPIN; ++sp) {
        __builtin_amdgcn_s_sleep(1);
        H2_ISSUE(vb, cb);
        H2_CHECK(va, ca);
        if (need == 0u) break;
        __builtin_amdgcn_s_sleep(1);
        H2_ISSUE(va, ca);
        H2_CHECK(vb, cb);
        if (need == 0u) break;
      }
      float s0 = (t0[0] + t0[1]) + (t0[2] + t0[3]), s1 = (t1[0] + t1[1]) + (t1[2] + t1[3]);
      s0 += dpp<0x128>(s0); s1 += dpp<0x128>(s1);
      s0 = xrow_sum(s0); s1 = xrow_sum(s1);
      if (lane < 8) ((GAS unsigned*)a.out)[bid * 8 + lane] = cvtpk(s0, s1);
      if (bid == 0 && lane == 0) __hip_atomic_store(ws64 + W_EPOCH / 8, T64, RLX_AGENT);
    }
    return;
  }

  if (c == 0) {
    lds_wait_ge(CT + C_TAGOK, 1u);
    T64 = ((u64)CT[C_TAG + 1] << 32) | CT[C_TAG];
    lds_wait_ge(CT + C_LANDED, 1u);
    LAS unsigned char* sl = L + L_A + (8 * g + (r >> 2)) * 64 + (r & 3) * 8;
    f32x4 d[2] = {{0.f, 0.f, 0.f, 0.f}, {0.f, 0.f, 0.f, 0.f}};
#pragma unroll
    for (int ks = 0; ks < 8; ++ks)
#pragma unroll
      for (int ct = 0; ct < 2; ++ct) {
        const v2u b0 = trread(sl + ks * 2048 + ct * 32), b1 = trread(sl + ks * 2048 + ct * 32 + 256);
        d[ct] = mfma(xf[ks], v4u{b0.x, b0.y, b1.x, b1.y}, d[ct]);
      }
    const f32x4 dv = g < 2 ? d[0] : d[1];
    gu64* qg = ws64 + W_QG / 8 + (size_t)((4 * (g & 1) * HKV + kap) * 2 + qk) * 512 + (qcol & 511) + (g >> 1) * 16 + r;
#pragma unroll
    for (int e = 0; e < 4; ++e) __hip_atomic_store(qg + (size_t)e * HKV * 1024, ((u64)TAG_HI << 32) | (f24(dv[e]) | TAG_LO), RLX_AGENT);
  } else if (c == 1) {
    lds_wait_ge(CT + C_TAGOK, 1u);
    T64 = ((u64)CT[C_TAG + 1] << 32) | CT[C_TAG];
    const v4u inv = {0u, ~(unsigned)T64, 0u, ~(unsigned)T64};
    const unsigned co = (unsigned)W_PG + (unsigned)(kap * 4 + (lane >> 5)) * PGB + (unsigned)(jg * 512 + (lane & 31) * 16);
    st16(rs, co, inv);
    st16(rs, co + 2u * PGB, inv);
    lds_wait_ge(CT + C_LANDED, 2u);
    LAS unsigned char* sl = L + L_RING + 7 * SLOT + g * 1024 + (r >> 2) * 64 + (r & 3) * 8;
    f32x4 d[4] = {{0.f, 0.f, 0.f, 0.f}, {0.f, 0.f, 0.f, 0.f}, {0.f, 0.f, 0.f, 0.f}, {0.f, 0.f, 0.f, 0.f}};
#pragma unroll
    for (int ks = 0; ks < 4; ++ks)
#pragma unroll
      for (int mc = 0; mc < 4; ++mc) {
        LAS unsigned char* q = sl + ks * 4096 + (mc >> 1) * 512 + (mc & 1) * 32;
        const v2u b0 = trread(q), b1 = trread(q + 256);
        d[mc] = mfma(xf[ks], v4u{b0.x, b0.y, b1.x, b1.y}, d[mc]);
      }
    LDS_WAIT();
    CT[C_GUFREE] = 1u;
    if (has_ffn) {
      const f32x4 dg = g < 2 ? d[0] : d[1], du = g < 2 ? d[2] : d[3];
      GAS unsigned* gp = (GAS unsigned*)(a.ws + W_GP + (size_t)(fcg * 4 + 2 * (g >> 1) + (g & 1)) * 2048 + fk * 512) + r;
#pragma unroll
      for (int e = 0; e < 4; ++e) {
        __hip_atomic_store(gp + e * 16, __float_as_uint(dg[e]), RLX_AGENT);
        __hip_atomic_store(gp + 64 + e * 16, __float_as_uint(du[e]), RLX_AGENT);
      }
    }
    VMWAIT(0);
    if (lane == 0) __hip_atomic_store(ws64 + W_FLAG / 8 + bid, T64, RLX_AGENT);
  } else {
    lds_wait_ge(CT + C_TAGOK, 1u);
    T64 = ((u64)CT[C_TAG + 1] << 32) | CT[C_TAG];
  }
  const unsigned T32 = (unsigned)T64;
  const v4u ones = {0x3F803F80u, 0x3F803F80u, 0x3F803F80u, 0x3F803F80u};
  LAS unsigned char* kbase = L + L_RING + c * SLOT + r * 256;
  LAS unsigned char* vbase = L + L_RING + (4 + c) * SLOT + (4 * g + (r >> 2)) * 256 + (r & 3) * 8;
  const int vx = 4 * (g & 1) + (r >> 2);

  v4u qf[4];
  f32x4 acc[8], accl = {0.f, 0.f, 0.f, 0.f};
#pragma unroll
  for (int m = 0; m < 8; ++m) acc[m] = f32x4{0.f, 0.f, 0.f, 0.f};
  float mrun = -INFINITY, fown = 0.f;
  v4u mlw = {0u, 0u, 0u, 0u};
  f32x4 basea = {0.f, 0.f, 0.f, 0.f}, baseb = {0.f, 0.f, 0.f, 0.f};
#pragma unroll 1
  for (int lap = 0; lap < NTILE / 4; ++lap) {
    const unsigned n = 4u * lap + c;
    lds_wait_ge(CT + C_LANDED, 3u + 2u * n);
    v4u kf[4][4];
#pragma unroll
    for (int T = 0; T < 4; ++T)
#pragma unroll
      for (int ks = 0; ks < 4; ++ks) kf[T][ks] = *(const LAS v4u*)(kbase + T * 4096 + (((4 * ks + g) ^ r) * 16));
    LDS_WAIT();
    CT[C_DONE + c] = (unsigned)(2 * lap + 1);
#pragma unroll
    for (int T = 0; T < 4; ++T) asm volatile("" : "+v"(kf[T][0]), "+v"(kf[T][1]), "+v"(kf[T][2]), "+v"(kf[T][3]) :: "memory");
    v4u vf[2][8];
#define READ_V() do { \
      lds_wait_ge(CT + C_LANDED, 4u + 2u * n); \
      _Pragma("unroll") for (int ks = 0; ks < 2; ++ks) \
        _Pragma("unroll") for (int m = 0; m < 8; ++m) { \
          LAS unsigned char* p = vbase + ks * 8192 + ((m ^ vx) * 32); \
          const v2u t0 = trread(p), t1 = trread(p + 4096); \
          vf[ks][m] = v4u{t0.x, t0.y, t1.x, t1.y}; \
        } \
      LDS_WAIT(); \
      CT[C_DONE + c] = (unsigned)(2 * lap + 2); \
      _Pragma("unroll") for (int ks = 0; ks < 2; ++ks) { \
        asm volatile("" : "+v"(vf[ks][0]), "+v"(vf[ks][1]), "+v"(vf[ks][2]), "+v"(vf[ks][3]) :: "memory"); \
        asm volatile("" : "+v"(vf[ks][4]), "+v"(vf[ks][5]), "+v"(vf[ks][6]), "+v"(vf[ks][7]) :: "memory"); \
      } } while (0)
    if (lap == 0) {
      READ_V();
      lds_wait_ge(CT + C_QRDY, 1u);
#pragma unroll
      for (int ks = 0; ks < 4; ++ks) qf[ks] = *(const LAS v4u*)(L + L_Q + ((r >> 2) * 128 + 32 * ks + 8 * g) * 2);
    }
    f32x4 sc[4];
#pragma unroll
    for (int T = 0; T < 4; ++T) {
      f32x4 d = {0.f, 0.f, 0.f, 0.f};
#pragma unroll
      for (int ks = 0; ks < 4; ++ks) d = mfma(kf[T][ks], qf[ks], d);
      sc[T] = d;
    }
    float tm = fmaxf(fmaxf(sc[0][0], sc[0][1]), fmaxf(sc[0][2], sc[0][3]));
#pragma unroll
    for (int T = 1; T < 4; ++T) tm = fmaxf(tm, fmaxf(fmaxf(sc[T][0], sc[T][1]), fmaxf(sc[T][2], sc[T][3])));
    tm = xrow_max(tm);
    if (__any(tm > mrun)) {
      const float mn = fmaxf(mrun, tm);
      const float al = __builtin_amdgcn_exp2f((mrun - mn) * SC2);
#pragma unroll
      for (int m = 0; m < 8; ++m) acc[m] *= al;
      accl *= al;
      mrun = mn;
    }
    const float mo = -mrun * SC2;
    v4u pf[2];
#pragma unroll
    for (int T = 0; T < 4; ++T) {
      float p[4];
#pragma unroll
      for (int e = 0; e < 4; ++e) p[e] = __builtin_amdgcn_exp2f(fmaf(sc[T][e], SC2, mo));
      pf[T >> 1][(T & 1) * 2] = cvtpk(p[0], p[1]);
      pf[T >> 1][(T & 1) * 2 + 1] = cvtpk(p[2], p[3]);
    }
    accl = mfma(ones, pf[0], accl);
    accl = mfma(ones, pf[1], accl);
    asm volatile("" : "+v"(pf[0]), "+v"(pf[1]), "+v"(accl) :: "memory");
    if (n == NTILE - 1) {
      float mw[3], lw[3]; f32x4 oa[3], ob[3];
#pragma unroll
      for (int w = 0; w < 3; ++w) {
        lds_wait_ge(CT + C_MRG + w, 1u);
        const LAS float* rw = (const LAS float*)(L + L_RING + w * SLOT);
        mw[w] = rw[512 + (r >> 2)]; lw[w] = rw[516 + (r >> 2)];
        oa[w] = *(const LAS f32x4*)(rw + r * 32 + 4 * g); ob[w] = *(const LAS f32x4*)(rw + r * 32 + 16 + 4 * g);
      }
      const unsigned m24 = f24(fmaxf(fmaxf(mrun, mw[0]), fmaxf(mw[1], mw[2])) * SC2);
      const float Mr = __uint_as_float(m24 << 8);
      fown = __builtin_amdgcn_exp2f(fmaf(mrun, SC2, -Mr));
      float Ls = fown * accl[0];
#pragma unroll
      for (int w = 0; w < 3; ++w) {
        const float f = __builtin_amdgcn_exp2f(fmaf(mw[w], SC2, -Mr));
        Ls = fmaf(f, lw[w], Ls); basea += f * oa[w]; baseb += f * ob[w];
      }
      const float inv = 1.f / Ls;
      fown *= inv; basea *= inv; baseb *= inv;
      mlw = v4u{m24 | TAG_LO, TAG_HI, f24(Ls) | TAG_LO, TAG_HI};
    }
    if (lap) READ_V();
#pragma unroll
    for (int ks = 0; ks < 2; ++ks)
#pragma unroll
      for (int m = 0; m < 8; ++m) acc[m] = mfma(vf[ks][m], pf[ks], acc[m]);
  }
  if (c == 3) {
    const int u = r & 3;
    const f32x4 sa = u == 0 ? acc[0] : u == 1 ? acc[2] : u == 2 ? acc[4] : acc[6], sb = u == 0 ? acc[1] : u == 1 ? acc[3] : u == 2 ? acc[5] : acc[7];
    const f32x4 xa = fown * sa + basea, xb = fown * sb + baseb;
    const unsigned po = (unsigned)W_PG + (unsigned)(kap * 4 + (r >> 2)) * PGB + (unsigned)(jg * 512 + g * 64 + u * 16);
    const v2u pa = {cvtpk(xa[0], xa[1]), cvtpk(xa[2], xa[3])}, pb = {cvtpk(xb[0], xb[1]), cvtpk(xb[2], xb[3])};
    st16(rs, po, v4u{pa.x, T32, pa.y, T32});
    st16(rs, po + 256u, v4u{pb.x, T32, pb.y, T32});
    if ((lane & 51) == 0) st16(rs, (unsigned)W_PG + (unsigned)(kap * 4 + (r >> 2)) * PGB + (unsigned)(16384 + jg * 128), mlw);
    if ((r >> 2) == hh) {
      LAS unsigned char* own = L + L_RING + (jg >> 3) * SLOT + 4096 + (jg & 7) * STG_ROW + 64 * u + 8 * g;
      *(LAS v2u*)own = pa; *(LAS v2u*)(own + 32) = pb;
      if ((lane & 51) == 0) *(LAS v2u*)(L + L_ML + jg * 8) = v2u{mlw.x << 8, mlw.z << 8};
    }
    CT[C_PPUB] = 1u;
  } else {
    LAS float* rec = (LAS float*)(L + L_RING + c * SLOT);
    if ((r & 3) == 0) {
#pragma unroll
      for (int m = 0; m < 8; ++m) *(LAS f32x4*)(rec + (r >> 2) * 128 + 16 * m + 4 * g) = acc[m];
      if (g == 0) { rec[512 + (r >> 2)] = mrun; rec[516 + (r >> 2)] = accl[0]; }
    }
    LDS_WAIT();
    CT[C_MRG + c] = 1u;
  }
  lds_wait_ge(CT + C_CLEAN, 1u);
  {
    LAS unsigned* stw = (LAS unsigned*)(L + L_RING + c * SLOT + 4096) + 16 * ((lane >> 1) & 3) + 8 * (lane >> 5) + 2 * ((lane >> 3) & 3) + (lane & 1);
    gu64* pgp = (gu64*)(a.ws + W_PG + (size_t)ht * PGB + c * 4096);
    gu64* dmy = ws64 + W_QG / 8 + (size_t)bid * 256;
    gu64* mlp = (gu64*)(a.ws + W_PG + (size_t)ht * PGB + 16384 + (8 * c + ((lane >> 1) & 7)) * 128) + (lane & 1);
    unsigned need = c == (jg >> 3) ? 0x1ffu & ~(1u << (jg & 7)) : 0x1ffu, ca = 0u, cb = 0u;
    u64 va[9], vb[9];
#define H1_ISSUE(v, cm) do { cm = need; \
      _Pragma("unroll") for (int i = 0; i < 8; ++i) v[i] = __hip_atomic_load(((cm >> i) & 1u) ? pgp + i * 64 + lane : dmy, RLX_AGENT); \
      v[8] = __hip_atomic_load(!mlok ? mlp : dmy, RLX_AGENT); } while (0)
#define H1_CHECK(v, cm) do { _Pragma("unroll") for (int i = 0; i < 8; ++i) \
      if (((cm & need) >> i) & 1u) { if (__all((unsigned)(v[i] >> 32) == T32)) { stw[i * (STG_ROW / 4)] = (unsigned)v[i]; need &= ~(1u << i); } } \
      if (!mlok && TAG_OK((unsigned)v[8], (unsigned)(v[8] >> 32))) { ((LAS unsigned*)(L + L_ML))[16 * c + lane] = (unsigned)v[8] << 8; mlok = true; } \
      if (__all(mlok)) need &= 0xffu; } while (0)
    bool mlok = lane >= 16 || (c == (jg >> 3) && (lane >> 1) == (jg & 7));
    v4u bw[4]; f32x4 dff = {0.f, 0.f, 0.f, 0.f};
    bool pre = false;
#define PRE_WORK() do { \
      LAS unsigned char* wb = L + L_A + (8 * g + (r >> 2)) * 128 + c * 32 + (r & 3) * 8; \
      _Pragma("unroll") for (int ks = 0; ks < 4; ++ks) { const v2u t0 = trread(wb + ks * 4096), t1 = trread(wb + ks * 4096 + 512); bw[ks] = v4u{t0.x, t0.y, t1.x, t1.y}; } \
      _Pragma("unroll") for (int ks = 0; ks < 2; ++ks) { \
        const v2u t0 = trread(wb + SLOT + ks * 4096), t1 = trread(wb + SLOT + ks * 4096 + 512); \
        const v4u hf = *(const LAS v4u*)(L + L_HB + (4 * (r & 1) + (r >> 2)) * HB_ROW + 64 * ks + 16 * g); \
        dff = mfma(hf, v4u{t0.x, t0.y, t1.x, t1.y}, dff); \
      } \
      asm volatile("" : "+v"(bw[0]), "+v"(bw[1]), "+v"(bw[2]), "+v"(bw[3]), "+v"(dff) :: "memory"); } while (0)
    lds_wait_ge(CT + C_PPUB, 1u);
    {
      const unsigned tgo = CT[C_TGO];
#pragma unroll 1
      for (int sp = 0; sp < 256 && (int)((unsigned)__builtin_amdgcn_s_memrealtime() - tgo) < 0; ++sp) __builtin_amdgcn_s_sleep(1);
    }
    H1_ISSUE(va, ca);
    _Pragma("unroll 1") for (unsigned sp = 0; sp < GSPIN; ++sp) {
      if (__builtin_popcount(need) <= H1_DIRECT) __builtin_amdgcn_s_sleep(1); else __builtin_amdgcn_s_sleep(8);
      H1_ISSUE(vb, cb);
      if (!pre && CT[C_WOK] != 0u) { CBAR(); PRE_WORK(); pre = true; }
      H1_CHECK(va, ca);
      if (need == 0u) break;
      if (__builtin_popcount(need) <= H1_DIRECT) __builtin_amdgcn_s_sleep(1); else __builtin_amdgcn_s_sleep(8);
      H1_ISSUE(va, ca);
      H1_CHECK(vb, cb);
      if (need == 0u) break;
    }
    LDS_WAIT();
    CT[C_STG + c] = 1u;
    if (!pre) { lds_wait_ge(CT + C_WOK, 1u); PRE_WORK(); }
#pragma unroll 1
    for (unsigned sp = 0; sp < LSPIN; ++sp) { const v4u fl = *(volatile LAS v4u*)(CT + C_STG); if (fl.x & fl.y & fl.z & fl.w) break; }
    CBAR();
    const LAS float* mls = (const LAS float*)(L + L_ML);
    LAS unsigned char* stg = L + L_RING + (r >> 3) * SLOT + 4096 + (r & 7) * STG_ROW + 16 * g;
    unsigned y[2];
#pragma unroll
    for (int rt = 0; rt < 2; ++rt) {
      f32x4 d = {0.f, 0.f, 0.f, 0.f};
#pragma unroll
      for (int ks = 0; ks < 4; ++ks) d = mfma(*(const LAS v4u*)(stg + 2 * rt * SLOT + 64 * ks), bw[ks], d);
      const f32x4 m01 = *(const LAS f32x4*)(mls + 32 * rt + 8 * g), m23 = *(const LAS f32x4*)(mls + 32 * rt + 8 * g + 4);
      const float Mx = fmaxf(fmaxf(m01[0], m01[2]), fmaxf(m23[0], m23[2]));
      const float w0 = __builtin_amdgcn_exp2f(m01[0] - Mx) * m01[1], w1 = __builtin_amdgcn_exp2f(m01[2] - Mx) * m01[3];
      const float w2 = __builtin_amdgcn_exp2f(m23[0] - Mx) * m23[1], w3 = __builtin_amdgcn_exp2f(m23[2] - Mx) * m23[3];
      const float gate = __uint_as_float(CT[C_G + 4 * rt + g]);
      const float att = ((w0 * d[0] + w1 * d[1]) + (w2 * d[2] + w3 * d[3])) / ((w0 + w1) + (w2 + w3));
      y[rt] = f24(fmaf(gate, att, (1.f - gate) * dff[rt])) | TAG_LO;
    }
    const unsigned snd = (r & 1) ? y[0] : y[1];
    const unsigned rcv = (unsigned)__builtin_amdgcn_mov_dpp((int)snd, 0xB1, 0xf, 0xf, true);
    st16(rs, (unsigned)W_OG + (unsigned)((((4 * (r & 1) + g) * 32 + 4 * n8 + c) * 32 + ht) * 16 + (r & 14)) * 8u,
         (r & 1) ? v4u{rcv, TAG_HI, y[1], TAG_HI} : v4u{y[0], TAG_HI, rcv, TAG_HI});
  }
}
}

extern "C" void kernel_launch(void* const* d_in, const int* in_sizes, int n_in, void* d_out, int out_size, void* d_ws,
                              size_t ws_size, hipStream_t stream) {
  (void)in_sizes; (void)n_in; (void)out_size;
  if (ws_size < W_END) return;
  Args a;
  a.x = (const unsigned char*)d_in[0]; a.k = (const unsigned char*)d_in[1]; a.v = (const unsigned char*)d_in[2];
  a.wq = (const unsigned char*)d_in[3]; a.wo = (const unsigned char*)d_in[4];
  a.wg = (const unsigned char*)d_in[6]; a.wu = (const unsigned char*)d_in[7]; a.wd = (const unsigned char*)d_in[8];
  a.gw = (const unsigned char*)d_in[9];
  a.out = (unsigned char*)d_out; a.ws = (unsigned char*)d_ws;
  (void)hipFuncSetAttribute((const void*)fused_kernel, hipFuncAttributeMaxDynamicSharedMemorySize, L_BYTES);
  hipLaunchKernelGGL(fused_kernel, dim3(NWG), dim3(NTHR), L_BYTES, stream, a);
}
```

```cpp
#include <hip/hip_runtime.h>
#include <hip/hip_bf16.h>
#include <stdint.h>

#define GAS __attribute__((address_space(1)))
#define LAS __attribute__((address_space(3)))
typedef unsigned v4u __attribute__((ext_vector_type(4)));
typedef unsigned v2u __attribute__((ext_vector_type(2)));
typedef float f32x4 __attribute__((ext_vector_type(4)));
typedef float f32x2 __attribute__((ext_vector_type(2)));
typedef __bf16 bf16x8 __attribute__((ext_vector_type(8)));
typedef short s16x4 __attribute__((ext_vector_type(4)));
typedef unsigned long long u64;
typedef GAS u64 gu64;
#define RLX_AGENT __ATOMIC_RELAXED, __HIP_MEMORY_SCOPE_AGENT

namespace {
constexpr int NB = 8, DM = 512, HQ = 32, HKV = 8, SK = 8192, DH = 128, FF = 1792;
constexpr int NWG = 256, NTHR = 6 * 64;
constexpr int NTILE = 32;
#define SW 16
#define WO_AT 32
#define H1_DIRECT 4
#define TPAT 4
#define PACE_R 550
#define PACE_A 90
#define PACE_D 44
#define PACE_G0 0.5f
#define H1_GO 170
#define H2_GO 0
#define QG_AT 6u
#define H1_SLEEP 1
#define H2_SLEEP 1
#define ML_SLEEP 8
constexpr int SLOT = 16384;
constexpr int L_RING = 0, L_A = 8 * SLOT, L_B = L_A + SLOT, L_Q = L_B + 8192, L_HB = L_Q + 1024, L_ML = L_HB + 2048, L_WML = L_ML + 128, L_CTRL = L_ML + 256, L_BYTES = L_CTRL + 256;
constexpr int STG_ROW = 272;
constexpr int HB_ROW = 144;
constexpr int C_LANDED = 0, C_GUFREE = 1, C_QRDY = 2, C_SEEN = 3, C_DONE = 4, C_MRG = 8, C_STG = 12, C_TAG = 16, C_TAGOK = 18, C_WOK = 19, C_CLEAN = 20, C_LAG = 21, C_PB = 22, C_PR = 23, C_G = 24, C_P0 = 32, C_TGO = 33, C_WGT = 34, C_KML = 36;
static_assert(L_BYTES <= 160 * 1024, "LDS");
constexpr size_t W_EPOCH = 0, W_G = 256, W_FLAG = 4096, W_LAG = 8192;
constexpr size_t W_QG = 16384;
constexpr size_t W_GP = W_QG + (size_t)2 * NB * 4096 * 8;
constexpr size_t W_HG = W_GP + (size_t)224 * 2048;
constexpr size_t W_PG = W_HG + (size_t)NB * FF * 8;
constexpr unsigned PGB = 16384 + 512;
constexpr size_t W_OG = W_PG + (size_t)HQ * PGB;
constexpr size_t W_END = W_OG + (size_t)NWG * 4096;
static_assert(W_END < (1u << 30) && W_PG % 1024 == 0, "buffer offsets are int");
constexpr u64 EPOCH_STEP = 0x9E3779B97F4A7C15ull;
constexpr unsigned GSPIN = 1u << 15, LSPIN = 1u << 18;
constexpr float SC2 = 0.08838834764831845f * 1.4426950408889634f;

struct Args {
  const unsigned char *x, *k, *v, *wq, *wo, *wg, *wu, *wd, *gw;
  unsigned char *out, *ws;
};

__device__ __forceinline__ f32x4 mfma(v4u a, v4u b, f32x4 c) {
  return __builtin_amdgcn_mfma_f32_16x16x32_bf16(__builtin_bit_cast(bf16x8, a), __builtin_bit_cast(bf16x8, b), c, 0, 0, 0);
}
__device__ __forceinline__ v2u trread(LAS unsigned char* p) {
  return __builtin_bit_cast(v2u, __builtin_amdgcn_ds_read_tr16_b64_v4i16((LAS s16x4*)p));
}
__device__ __forceinline__ unsigned cvtpk(float lo, float hi) {
  typedef __bf16 bf16x2 __attribute__((ext_vector_type(2)));
  bf16x2 r; r[0] = (__bf16)lo; r[1] = (__bf16)hi; return __builtin_bit_cast(unsigned, r);
}
template <int CTRL> __device__ __forceinline__ float dpp(float x) {
  return __builtin_bit_cast(float, __builtin_amdgcn_mov_dpp(__builtin_bit_cast(int, x), CTRL, 0xf, 0xf, true));
}
__device__ __forceinline__ float xrow_max(float x) {
  auto s = __builtin_amdgcn_permlane16_swap(__float_as_uint(x), __float_as_uint(x), false, false);
  x = fmaxf(__uint_as_float(s[0]), __uint_as_float(s[1]));
  auto t = __builtin_amdgcn_permlane32_swap(__float_as_uint(x), __float_as_uint(x), false, false);
  return fmaxf(__uint_as_float(t[0]), __uint_as_float(t[1]));
}
__device__ __forceinline__ float xrow_sum(float x) {
  auto s = __builtin_amdgcn_permlane16_swap(__float_as_uint(x), __float_as_uint(x), false, false);
  x = __uint_as_float(s[0]) + __uint_as_float(s[1]);
  auto t = __builtin_amdgcn_permlane32_swap(__float_as_uint(x), __float_as_uint(x), false, false);
  return __uint_as_float(t[0]) + __uint_as_float(t[1]);
}
__device__ __forceinline__ float bflo(unsigned u) { return __uint_as_float(u << 16); }
__device__ __forceinline__ float bfhi(unsigned u) { return __uint_as_float(u & 0xffff0000u); }

__device__ __forceinline__ v4u ld16(__amdgpu_buffer_rsrc_t rs, unsigned off) { return __builtin_amdgcn_raw_buffer_load_b128(rs, (int)off, 0, 16); }
__device__ __forceinline__ void st16(__amdgpu_buffer_rsrc_t rs, unsigned off, v4u v) { __builtin_amdgcn_raw_buffer_store_b128(v, rs, (int)off, 0, 16); }
__device__ __forceinline__ unsigned f24(float v) { return (__float_as_uint(v) + 0x80u) >> 8; }
#define TAG_LO ((unsigned)T64 << 24)
#define TAG_HI ((unsigned)(T64 >> 8))
#define TAG_OK(lo, hi) ((hi) == TAG_HI && ((lo) ^ TAG_LO) < 0x1000000u)
#define CBAR() asm volatile("" ::: "memory")
#define LDS_WAIT() asm volatile("s_waitcnt lgkmcnt(0)" ::: "memory")
#define KPOL " nt"
#define VPOL " nt"
#define WPOL " nt"
#define DMA4(sbase, dst, v0, v1, v2, v3, POL)                                                                    \
  asm volatile("s_mov_b32 m0, %5\n\ts_nop 0\n\t"                                                                 \
               "global_load_lds_dwordx4 %0, %4" POL "\n\t"                                                       \
               "global_load_lds_dwordx4 %1, %4 offset:1024" POL "\n\t"                                           \
               "global_load_lds_dwordx4 %2, %4 offset:2048" POL "\n\t"                                           \
               "global_load_lds_dwordx4 %3, %4 offset:3072" POL                                                  \
               :: "v"(v0), "v"(v1), "v"(v2), "v"(v3), "s"(sbase), "s"(dst) : "memory")
#define DMA1S(sbase, voff, dst)                                                                                  \
  asm volatile("s_mov_b32 m0, %2\n\ts_nop 0\n\tglobal_load_lds_dwordx4 %0, %1" WPOL :: "v"(voff), "s"(sbase), "s"(dst) : "memory")
#define DMA1V(vaddr, dst)                                                                                        \
  asm volatile("s_mov_b32 m0, %1\n\ts_nop 0\n\tglobal_load_lds_dwordx4 %0, off" WPOL :: "v"(vaddr), "s"(dst) : "memory")
#define VMWAIT(n) asm volatile("s_waitcnt vmcnt(" #n ")" ::: "memory")
#define VMWAITN(n) asm volatile("s_waitcnt vmcnt(%0)" :: "i"(n) : "memory")
#define TS(i) do { } while (0)
#define TS_FLUSH(w) do { } while (0)

__device__ __forceinline__ void lds_wait_ge(volatile LAS unsigned* w, unsigned need) {
#pragma unroll 1
  for (unsigned sp = 0; *w < need && sp < LSPIN; ++sp) __builtin_amdgcn_s_sleep(1);
  CBAR();
}

__global__ void __launch_bounds__(NTHR, 2) fused_kernel(Args a) {
  extern __shared__ __attribute__((aligned(16))) unsigned char smem[];
  LAS unsigned char* L = (LAS unsigned char*)smem;
  volatile LAS unsigned* CT = (volatile LAS unsigned*)(L + L_CTRL);
  const int tid = threadIdx.x, lane = tid & 63, wave = __builtin_amdgcn_readfirstlane(tid >> 6);
  const int bid = blockIdx.x;
  const int kap = bid >> 5, jg = bid & 31, b = jg >> 2, s = jg & 3;
  const int qcol = kap * 512 + (((jg & 7) << 1) | ((jg >> 3) & 1)) * 32, qk = jg >> 4;
  const bool has_ffn = (bid >> 3) < 28;
  const int fi = has_ffn ? (bid >> 3) : 27;
  const int fcg = (bid & 7) * 7 + (fi >> 2), fk = fi & 3;
  const int hh = jg >> 3, ht = kap * 4 + hh, n8 = jg & 7;
  const int g = lane >> 4, r = lane & 15;
  gu64* ws64 = (gu64*)a.ws;
  const __amdgpu_buffer_rsrc_t rs = __builtin_amdgcn_make_buffer_rsrc((void*)a.ws, 0, (int)W_END, 0x00020000);
  if (__builtin_expect(wave == 0, 1)) {
    __builtin_amdgcn_s_waitcnt(0);
    __builtin_amdgcn_s_setprio(3);
    const unsigned lb = (unsigned)(size_t)L;
    {
      const unsigned voff = (unsigned)(lane >> 2) * 8192u + (unsigned)(lane & 3) * 16u;
      const unsigned char* sb = a.wq + (size_t)qk * 256 * 8192 + (size_t)qcol * 2;
#pragma unroll 1
      for (int j = 0; j < 16; ++j) DMA1S(sb + (size_t)j * 16 * 8192, voff, lb + L_A + j * 1024);
    }
    {
      const unsigned char* p = ((lane & 32) ? a.wu : a.wg) + (size_t)(fk * 128 + ((lane >> 2) & 7)) * (FF * 2) + (size_t)fcg * 64 + (lane & 3) * 16;
#pragma unroll 1
      for (int j = 0; j < 16; ++j) DMA1V(p + (size_t)j * 8 * (FF * 2), lb + L_RING + 7 * SLOT + j * 1024);
    }
    __builtin_amdgcn_s_barrier();
    CBAR();
    unsigned ko[4], vo[2];
#pragma unroll
    for (int j = 0; j < 4; ++j) ko[j] = (unsigned)g * 256u + (unsigned)((r ^ g ^ (4 * j)) * 16);
#pragma unroll
    for (int j = 0; j < 2; ++j) vo[j] = (unsigned)g * 256u + (unsigned)((r ^ (2 * (4 * j + g))) * 16);
    const size_t seq_off = (size_t)(b * HKV + kap) * SK * 256;
    const unsigned char* kb = a.k + seq_off;
    const unsigned char* vb = a.v + seq_off;
#define LOAD_WO(last) do { \
      const unsigned voff = (unsigned)(lane >> 3) * 1024u + (unsigned)(lane & 7) * 16u; \
      const unsigned char* sb = a.wo + (size_t)ht * 128 * 1024 + (size_t)n8 * 128; \
      const unsigned char* sd = a.wd + (size_t)ht * 56 * 1024 + (size_t)n8 * 128; \
      _Pragma("unroll 1") for (int j = 0; j < 23; ++j) { \
        DMA1S(j < 16 ? sb + (size_t)j * 8192 : sd + (size_t)(j - 16) * 8192, voff, lb + L_A + j * 1024); \
        if (last) { if (j == 7) { VMWAIT(8); CT[C_LANDED] = 2u + 2u * NTILE; } } \
        else if ((j & 7) == 7) VMWAIT(16); \
      } } while (0)
    unsigned seen = 0;
    unsigned pb = (unsigned)__builtin_amdgcn_s_memrealtime() + (unsigned)(PACE_A - PACE_D);
    int pr = PACE_R;
    CT[C_P0] = pb;
    bool deep = true;
#pragma unroll 1
    for (int hv = 0; hv < 2; ++hv) {
      const unsigned o0 = hv ? vo[0] : ko[0], o1 = hv ? vo[1] : ko[1], o2 = hv ? vo[0] : ko[2], o3 = hv ? vo[1] : ko[3];
      const unsigned char* xb = hv ? vb : kb;
#pragma unroll 1
      for (int n = 0; n < NTILE; ++n) {
        const int tile = ((n + 4 * b) & 31) * 4 + s;
        const int m = hv * NTILE + n;
        const unsigned f = 2u + m;
        if (m >= 7) {
          volatile LAS unsigned* fw = m >= 8 ? (CT + C_DONE + (m & 3)) : (CT + C_GUFREE);
          const unsigned need = m >= 8 ? (unsigned)((m >> 2) - 1) : 1u;
          if (m < 8 || seen < need) {
            if (*fw < need) {
              VMWAIT(0); CT[C_LANDED] = f;
              lds_wait_ge(fw, need);
            }
          }
        }
        const unsigned char* sb = xb + (size_t)tile * SLOT;
        const unsigned dst = lb + L_RING + (m & 7) * SLOT;
        DMA4(sb, dst, o0, o1, o2, o3, KPOL); DMA4(sb + 4096, dst + 4096, o0, o1, o2, o3, KPOL);
        if (deep) VMWAIT(32); else VMWAIT(16);
        DMA4(sb + 8192, dst + 8192, o0, o1, o2, o3, KPOL); DMA4(sb + 12288, dst + 12288, o0, o1, o2, o3, KPOL);
        const unsigned nx = CT[C_DONE + ((m + 1) & 3)];
        const unsigned now = (unsigned)__builtin_amdgcn_s_memrealtime();
        const v2u ps = *(volatile LAS v2u*)(CT + C_PB);
        if (deep) { VMWAIT(32); CT[C_LANDED] = f - 1; } else { VMWAIT(16); CT[C_LANDED] = f; }
        CT[C_LAG] = now - (unsigned)(PACE_R * (int)f / 10);
        if (__builtin_amdgcn_readfirstlane(ps.y)) { pb = __builtin_amdgcn_readfirstlane(ps.x); pr = __builtin_amdgcn_readfirstlane(ps.y); }
        deep = ((int)(now - pb - (unsigned)(pr * (int)f / 10)) > PACE_D && f < 2u * NTILE) || m == 0;
        seen = __builtin_amdgcn_readfirstlane(nx);
        if (f == 12) TS(1);
        if (f == 3) TS(5);
        if (f == 6) TS(6);
        if (f == 34) TS(7);
      }
    }
    CT[C_TGO] = pb + (unsigned)(pr * (2 * NTILE + 1) / 10 + H1_GO);
    {
      if (WO_AT < NTILE) {
        VMWAIT(16); CT[C_LANDED] = 1u + 2u * NTILE;
        VMWAIT(0); CT[C_LANDED] = 2u + 2u * NTILE;
        TS(2);
      } else {
        VMWAIT(16); CT[C_LANDED] = 1u + 2u * NTILE;
        LOAD_WO(true);
        TS(2);
        VMWAIT(0);
      }
      CT[C_WOK] = 1u;
      TS(3);
      TS_FLUSH(0);
    }
    return;
  }

  const int c = wave - 1;
  u64 T64 = 0;
  v4u xf[8];
  if (wave == 5) T64 = __hip_atomic_load(ws64 + W_EPOCH / 8, RLX_AGENT);
  if (wave <= 2) {
    const GAS v4u* xp = (const GAS v4u*)(a.x + (size_t)(r & 7) * 1024 + (wave == 1 ? qk * 512 : fk * 256) + g * 16);
#pragma unroll
    for (int ks = 0; ks < 8; ++ks) xf[ks] = xp[(wave == 1 ? ks : (ks & 3)) * 4];
  }
  if (wave == 3) ((LAS unsigned*)(L + L_CTRL))[lane] = 0u;
  if (wave == 4) ((LAS v4u*)(L + L_B + 7168))[lane] = v4u{0u, 0u, 0u, 0u};
  LDS_WAIT();
  __builtin_amdgcn_s_barrier();
  CBAR();

  if (wave == 5) {
    T64 += EPOCH_STEP;
    if (lane == 0) { CT[C_TAG] = (unsigned)T64; CT[C_TAG + 1] = (unsigned)(T64 >> 32); LDS_WAIT(); CT[C_TAGOK] = 1u; }
#define PACE_POST(k) do { if (lane == 0) __hip_atomic_store(ws64 + (W_LAG + (k) * 2048) / 8 + bid, (T64 << 32) | CT[C_LAG], RLX_AGENT); } while (0)
#define PACE_MEAN(k, m, ok) do { const unsigned p0 = CT[C_P0]; \
      const u64 lv = __hip_atomic_load(ws64 + (W_LAG + (k) * 2048) / 8 + kap * 32 + (lane & 31), RLX_AGENT); \
      const bool vk = (unsigned)(lv >> 32) == (unsigned)T64 && lane < 32; \
      int dl = vk ? (int)((unsigned)lv - p0) : 0; dl = dl > 2000 ? 2000 : dl < -2000 ? -2000 : dl; \
      float sm = (float)dl; sm += dpp<0xB1>(sm); sm += dpp<0x4E>(sm); sm += dpp<0x141>(sm); sm += dpp<0x140>(sm); sm = xrow_sum(sm); \
      const int cn = __builtin_popcountll(__ballot(vk)); ok = cn >= 24; m = sm / (float)(cn > 0 ? cn : 1); } while (0)
#define PACE_SET(m, f, dr) do { int nr = PACE_R + (int)(dr); nr = nr < 520 ? 520 : nr > 680 ? 680 : nr; \
      if (lane == 0) *(volatile LAS v2u*)(CT + C_PB) = v2u{CT[C_P0] + (unsigned)(int)(m) - (unsigned)((nr - PACE_R) * (f) / 10), (unsigned)nr}; } while (0)
    float m0; bool ok0;
    if (bid == NWG - 1) {
      const int gb = lane >> 3, part = lane & 7;
      const GAS v4u* xp = (const GAS v4u*)(a.x + (size_t)gb * 1024 + part * 128);
      const GAS v4u* gp = (const GAS v4u*)(a.gw + part * 128);
      float ga = 0.f;
#pragma unroll
      for (int i = 0; i < 8; ++i) {
        const v4u xv = xp[i], gv = gp[i];
#pragma unroll
        for (int e = 0; e < 4; ++e) { ga = fmaf(bflo(xv[e]), bflo(gv[e]), ga); ga = fmaf(bfhi(xv[e]), bfhi(gv[e]), ga); }
      }
      ga += dpp<0xB1>(ga); ga += dpp<0x4E>(ga); ga += dpp<0x141>(ga);
      const float gate = 1.f / (1.f + __expf(-ga));
      if (part == 0) __hip_atomic_store(ws64 + W_G / 8 + gb, ((u64)TAG_HI << 32) | (f24(gate) | TAG_LO), RLX_AGENT);
    }
    {
      lds_wait_ge(CT + C_LANDED, QG_AT);
      const unsigned qo = (unsigned)W_QG + (unsigned)((b * HKV + kap) * 8192 + lane * 16);
      _Pragma("unroll 1") for (unsigned sp = 0; sp < GSPIN; ++sp) {
        v4u v[8]; bool ok = true;
#pragma unroll
        for (int i = 0; i < 8; ++i) v[i] = ld16(rs, qo + i * 1024u);
#pragma unroll
        for (int i = 0; i < 4; ++i) {
          ok &= TAG_OK(v[i].x, v[i].y) && TAG_OK(v[i].z, v[i].w) && TAG_OK(v[i + 4].x, v[i + 4].y) && TAG_OK(v[i + 4].z, v[i + 4].w);
          ((LAS unsigned*)(L + L_Q))[i * 64 + lane] = cvtpk(__uint_as_float(v[i].x << 8) + __uint_as_float(v[i + 4].x << 8), __uint_as_float(v[i].z << 8) + __uint_as_float(v[i + 4].z << 8));
        }
        if (__all(ok)) break;
        __builtin_amdgcn_s_sleep(8);
      }
      LDS_WAIT(); CT[C_QRDY] = 1u;
      TS(2);
    }
    {
      lds_wait_ge(CT + C_LANDED, 12u);
      PACE_POST(0);
      const int fb = lane < 32 ? kap * 32 + lane : (bid & 7) + 8 * ((fi & ~3) + (lane & 3));
      _Pragma("unroll 1") for (unsigned sp = 0; sp < GSPIN; ++sp) {
        if (__all(__hip_atomic_load(ws64 + W_FLAG / 8 + fb, RLX_AGENT) == T64)) break;
        __builtin_amdgcn_s_sleep(32);
      }
      CBAR();
    }
    if (has_ffn) {
      const unsigned go = (unsigned)W_GP + (unsigned)((fcg * 4 + fk) * 2048 + lane * 16);
      const f32x4 p0 = __builtin_bit_cast(f32x4, ld16(rs, go)), p1 = __builtin_bit_cast(f32x4, ld16(rs, go + 1024u));
      unsigned hw[4];
#pragma unroll
      for (int e = 0; e < 4; ++e) {
        const unsigned t = __float_as_uint(p0[e] + p1[e]);
        auto s32 = __builtin_amdgcn_permlane32_swap(t, t, false, false);
        const unsigned tot = __float_as_uint(__uint_as_float(s32[0]) + __uint_as_float(s32[1]));
        auto s16 = __builtin_amdgcn_permlane16_swap(tot, tot, false, false);
        const float gt = __uint_as_float(s16[0]), up = __uint_as_float(s16[1]);
        const float u = 0.7978845608028654f * (gt + 0.044715f * gt * gt * gt);
        const float th = 1.f - 2.f / (__expf(2.f * u) + 1.f);
        hw[e] = f24(0.5f * gt * (1.f + th) * up) | TAG_LO;
      }
      const int src = ((2 * (g & 1) + (r >> 3)) * 4 + ((r & 7) >> 1)) * 4;
      const unsigned a0 = __builtin_amdgcn_ds_bpermute(src, (int)hw[0]), a1 = __builtin_amdgcn_ds_bpermute(src, (int)hw[1]);
      const unsigned a2 = __builtin_amdgcn_ds_bpermute(src, (int)hw[2]), a3 = __builtin_amdgcn_ds_bpermute(src, (int)hw[3]);
      if (lane < 32)
        st16(rs, (unsigned)W_HG + (unsigned)((4 * (fk & 1) + 2 * g + (r >> 3)) * FF + fcg * 32 + (fk >> 1) * 16 + 2 * (r & 7)) * 8u,
             v4u{(r & 1) ? a2 : a0, TAG_HI, (r & 1) ? a3 : a1, TAG_HI});
    }
    {
      lds_wait_ge(CT + C_LANDED, 20u);
      PACE_MEAN(0, m0, ok0);
      if (ok0) PACE_SET(m0, 12, m0 * PACE_G0);
      const bool pad = (lane & 31) >= 28;
      const unsigned ho = (unsigned)W_HG + (unsigned)((lane >> 5) * FF + ht * 56 + (pad ? 0 : 2 * (lane & 31))) * 8u;
      _Pragma("unroll 1") for (unsigned sp = 0; sp < GSPIN; ++sp) {
        v4u v[4];
#pragma unroll
        for (int i = 0; i < 4; ++i) v[i] = ld16(rs, ho + (unsigned)(i * 2 * FF * 8));
        const u64 gv = __hip_atomic_load(ws64 + W_G / 8 + (lane & 7), RLX_AGENT);
        bool ok = TAG_OK((unsigned)gv, (unsigned)(gv >> 32));
#pragma unroll
        for (int i = 0; i < 4; ++i) {
          ok &= TAG_OK(v[i].x, v[i].y) && TAG_OK(v[i].z, v[i].w);
          *(LAS unsigned*)(L + L_HB + (2 * i + (lane >> 5)) * HB_ROW + (lane & 31) * 4) =
              pad ? 0u : cvtpk(__uint_as_float(v[i].x << 8), __uint_as_float(v[i].z << 8));
        }
        if (lane < 8) CT[C_G + lane] = (unsigned)gv << 8;
        if (__all(ok)) break;
        __builtin_amdgcn_s_sleep(32);
      }
      LDS_WAIT();
      CT[C_CLEAN] = 1u;
      TS(1);
    }
    {
      float m1, m2; bool ok1, ok2;
      lds_wait_ge(CT + C_LANDED, 27u);
      PACE_POST(1);
      lds_wait_ge(CT + C_LANDED, 34u);
      PACE_MEAN(1, m1, ok1);
      if (ok0 && ok1) PACE_SET(m1, 27, (m1 - m0) * (10.f / 15.f));
      lds_wait_ge(CT + C_LANDED, 43u);
      PACE_POST(2);
      lds_wait_ge(CT + C_LANDED, 50u);
      PACE_MEAN(2, m2, ok2);
      if (ok1 && ok2) PACE_SET(m2, 43, (m2 - m1) * (10.f / 16.f));
    }
    {
      const unsigned mo = (unsigned)W_PG + (unsigned)ht * PGB + (unsigned)(16384 + (lane & 31) * 16);
      v4u mv;
      _Pragma("unroll 1") for (unsigned sp = 0; sp < GSPIN; ++sp) {
        mv = ld16(rs, mo);
        if (__all(TAG_OK(mv.x, mv.y) && TAG_OK(mv.z, mv.w))) break;
        __builtin_amdgcn_s_sleep(32);
      }
      const float ms = __uint_as_float(mv.x << 8);
      float mx = fmaxf(ms, dpp<0xB1>(ms)); mx = fmaxf(mx, dpp<0x4E>(mx));
      const float wl = __builtin_amdgcn_exp2f(ms - mx) * __uint_as_float(mv.z << 8);
      float lt = wl + dpp<0xB1>(wl); lt += dpp<0x4E>(lt);
      if (lane < 32) ((LAS float*)(L + L_ML))[lane] = __uint_as_float(CT[C_G + (lane >> 2)]) * wl / lt;
      LDS_WAIT();
      CT[C_WGT] = 1u;
    }
    TS(3);
    {
      const unsigned oo = (unsigned)W_OG + (unsigned)(bid * 4096), ol = (unsigned)lane * 16u;
      float t0[4], t1[4];
      v4u va[4], vb[4];
      unsigned need = 15u, ca, cb;
#define H2_ISSUE(v, cm) do { cm = need; _Pragma("unroll") for (int i = 0; i < 4; ++i) v[i] = ld16(rs, ((cm >> i) & 1u) ? oo + i * 1024u + ol : (unsigned)W_QG + (unsigned)bid * 2048u); } while (0)
#define H2_CHECK(v, cm) do { _Pragma("unroll") for (int i = 0; i < 4; ++i) if (((cm & need) >> i) & 1u) { \
        if (__all(TAG_OK(v[i].x, v[i].y) && TAG_OK(v[i].z, v[i].w))) { t0[i] = __uint_as_float(v[i].x << 8); t1[i] = __uint_as_float(v[i].z << 8); need &= ~(1u << i); } } } while (0)
#pragma unroll 1
      for (unsigned sp = 0; sp < LSPIN; ++sp) { const v4u fl = *(volatile LAS v4u*)(CT + C_STG); if (fl.x & fl.y & fl.z & fl.w) break; __builtin_amdgcn_s_sleep(1); }
      H2_ISSUE(va, ca);
      _Pragma("unroll 1") for (unsigned sp = 0; sp < GSPIN; ++sp) {
        __builtin_amdgcn_s_sleep(H2_SLEEP);
        H2_ISSUE(vb, cb);
        H2_CHECK(va, ca);
        if (need == 0u) break;
        __builtin_amdgcn_s_sleep(H2_SLEEP);
        H2_ISSUE(va, ca);
        H2_CHECK(vb, cb);
        if (need == 0u) break;
      }
      float s0 = (t0[0] + t0[1]) + (t0[2] + t0[3]), s1 = (t1[0] + t1[1]) + (t1[2] + t1[3]);
      s0 += dpp<0x128>(s0); s1 += dpp<0x128>(s1);
      s0 = xrow_sum(s0); s1 = xrow_sum(s1);
      if (lane < 8) ((GAS unsigned*)a.out)[bid * 8 + lane] = cvtpk(s0, s1);
      TS(4);
      if (bid == 0 && lane == 0) __hip_atomic_store(ws64 + W_EPOCH / 8, T64, RLX_AGENT);
    }
    TS_FLUSH(5);
    return;
  }

  if (c == 0) {
    lds_wait_ge(CT + C_TAGOK, 1u);
    T64 = ((u64)CT[C_TAG + 1] << 32) | CT[C_TAG];
    lds_wait_ge(CT + C_LANDED, 1u);
    TS(0);
    LAS unsigned char* sl = L + L_A + (8 * g + (r >> 2)) * 64 + (r & 3) * 8;
    f32x4 d[2] = {{0.f, 0.f, 0.f, 0.f}, {0.f, 0.f, 0.f, 0.f}};
#pragma unroll
    for (int ks = 0; ks < 8; ++ks)
#pragma unroll
      for (int ct = 0; ct < 2; ++ct) {
        const v2u b0 = trread(sl + ks * 2048 + ct * 32), b1 = trread(sl + ks * 2048 + ct * 32 + 256);
        d[ct] = mfma(xf[ks], v4u{b0.x, b0.y, b1.x, b1.y}, d[ct]);
      }
    const f32x4 dv = g < 2 ? d[0] : d[1];
    gu64* qg = ws64 + W_QG / 8 + (size_t)((4 * (g & 1) * HKV + kap) * 2 + qk) * 512 + (qcol & 511) + (g >> 1) * 16 + r;
#pragma unroll
    for (int e = 0; e < 4; ++e) __hip_atomic_store(qg + (size_t)e * HKV * 1024, ((u64)TAG_HI << 32) | (f24(dv[e]) | TAG_LO), RLX_AGENT);
    TS(1);
  } else if (c == 1) {
    lds_wait_ge(CT + C_TAGOK, 1u);
    T64 = ((u64)CT[C_TAG + 1] << 32) | CT[C_TAG];
    const v4u inv = {0u, ~(unsigned)T64, 0u, ~(unsigned)T64};
    const unsigned co = (unsigned)W_PG + (unsigned)(kap * 4 + (lane >> 5)) * PGB + (unsigned)(jg * 512 + (lane & 31) * 16);
    st16(rs, co, inv);
    st16(rs, co + 2u * PGB, inv);
    lds_wait_ge(CT + C_LANDED, 2u);
    LAS unsigned char* sl = L + L_RING + 7 * SLOT + g * 1024 + (r >> 2) * 64 + (r & 3) * 8;
    f32x4 d[4] = {{0.f, 0.f, 0.f, 0.f}, {0.f, 0.f, 0.f, 0.f}, {0.f, 0.f, 0.f, 0.f}, {0.f, 0.f, 0.f, 0.f}};
#pragma unroll
    for (int ks = 0; ks < 4; ++ks)
#pragma unroll
      for (int mc = 0; mc < 4; ++mc) {
        LAS unsigned char* q = sl + ks * 4096 + (mc >> 1) * 512 + (mc & 1) * 32;
        const v2u b0 = trread(q), b1 = trread(q + 256);
        d[mc] = mfma(xf[ks], v4u{b0.x, b0.y, b1.x, b1.y}, d[mc]);
      }
    LDS_WAIT();
    CT[C_GUFREE] = 1u;
    if (has_ffn) {
      const f32x4 dg = g < 2 ? d[0] : d[1], du = g < 2 ? d[2] : d[3];
      GAS unsigned* gp = (GAS unsigned*)(a.ws + W_GP + (size_t)(fcg * 4 + 2 * (g >> 1) + (g & 1)) * 2048 + fk * 512) + r;
#pragma unroll
      for (int e = 0; e < 4; ++e) {
        __hip_atomic_store(gp + e * 16, __float_as_uint(dg[e]), RLX_AGENT);
        __hip_atomic_store(gp + 64 + e * 16, __float_as_uint(du[e]), RLX_AGENT);
      }
    }
    VMWAIT(0);
    if (lane == 0) __hip_atomic_store(ws64 + W_FLAG / 8 + bid, T64, RLX_AGENT);
  } else {
    lds_wait_ge(CT + C_TAGOK, 1u);
    T64 = ((u64)CT[C_TAG + 1] << 32) | CT[C_TAG];
  }
  const unsigned T32 = (unsigned)T64;
  const v4u ones = {0x3F803F80u, 0x3F803F80u, 0x3F803F80u, 0x3F803F80u};
  const int vx = 4 * (g & 1) + (r >> 2);

  v4u qf[4], pq[8][2];
  float mq[8];
  f32x4 accl = {0.f, 0.f, 0.f, 0.f};
  float mrun = -INFINITY;
#pragma unroll
  for (int i = 0; i < 8; ++i) { pq[i][0] = v4u{0u, 0u, 0u, 0u}; pq[i][1] = pq[i][0]; mq[i] = 0.f; }
#define READ_K(dstk, lp) do { \
      lds_wait_ge(CT + C_LANDED, 3u + 4u * (lp) + c); \
      LAS unsigned char* kbase = L + L_RING + (c + 4 * ((lp) & 1)) * SLOT + r * 256; \
      _Pragma("unroll") for (int T = 0; T < 4; ++T) \
        _Pragma("unroll") for (int ks = 0; ks < 4; ++ks) dstk[T][ks] = *(const LAS v4u*)(kbase + T * 4096 + (((4 * ks + g) ^ r) * 16)); \
      LDS_WAIT(); \
      CT[C_DONE + c] = (unsigned)((lp) + 1); \
      _Pragma("unroll") for (int T = 0; T < 4; ++T) asm volatile("" : "+v"(dstk[T][0]), "+v"(dstk[T][1]), "+v"(dstk[T][2]), "+v"(dstk[T][3]) :: "memory"); } while (0)
  {
    v4u kf[4][4], kn[4][4];
    READ_K(kf, 0);
    READ_K(kn, 1);
    lds_wait_ge(CT + C_QRDY, 1u);
#pragma unroll
    for (int ks = 0; ks < 4; ++ks) qf[ks] = *(const LAS v4u*)(L + L_Q + ((r >> 2) * 128 + 32 * ks + 8 * g) * 2);
#pragma unroll 1
    for (int lap = 0; lap < NTILE / 4; ++lap) {
      if (lap == 1) {
#pragma unroll
        for (int T = 0; T < 4; ++T)
#pragma unroll
          for (int ks = 0; ks < 4; ++ks) kf[T][ks] = kn[T][ks];
      } else if (lap) READ_K(kf, lap);
      f32x4 sc[4];
#pragma unroll
      for (int T = 0; T < 4; ++T) {
        f32x4 d = {0.f, 0.f, 0.f, 0.f};
#pragma unroll
        for (int ks = 0; ks < 4; ++ks) d = mfma(kf[T][ks], qf[ks], d);
        sc[T] = d;
      }
      float tm = fmaxf(fmaxf(sc[0][0], sc[0][1]), fmaxf(sc[0][2], sc[0][3]));
#pragma unroll
      for (int T = 1; T < 4; ++T) tm = fmaxf(tm, fmaxf(fmaxf(sc[T][0], sc[T][1]), fmaxf(sc[T][2], sc[T][3])));
      tm = xrow_max(tm);
      const float mn = fmaxf(mrun, tm);
      accl *= __builtin_amdgcn_exp2f((mrun - mn) * SC2);
      mrun = mn;
      const float mo = -mrun * SC2;
#pragma unroll
      for (int i = 0; i < 7; ++i) { pq[i][0] = pq[i + 1][0]; pq[i][1] = pq[i + 1][1]; mq[i] = mq[i + 1]; }
      mq[7] = mrun;
#pragma unroll
      for (int T = 0; T < 4; ++T) {
        float p[4];
#pragma unroll
        for (int e = 0; e < 4; ++e) p[e] = __builtin_amdgcn_exp2f(fmaf(sc[T][e], SC2, mo));
        pq[7][T >> 1][(T & 1) * 2] = cvtpk(p[0], p[1]);
        pq[7][T >> 1][(T & 1) * 2 + 1] = cvtpk(p[2], p[3]);
      }
      accl = mfma(ones, pq[7][0], accl);
      accl = mfma(ones, pq[7][1], accl);
    }
  }
  float fown;
  {
    LAS float* ml = (LAS float*)(L + L_WML);
    if ((lane & 51) == 0) { ml[c * 8 + (r >> 2)] = mrun; ml[c * 8 + 4 + (r >> 2)] = accl[0]; }
    LDS_WAIT();
    CT[C_KML + c] = 1u;
#pragma unroll 1
    for (unsigned sp = 0; sp < LSPIN; ++sp) { const v4u fl = *(volatile LAS v4u*)(CT + C_KML); if (fl.x & fl.y & fl.z & fl.w) break; __builtin_amdgcn_s_sleep(1); }
    CBAR();
    float mw[4], lw[4];
#pragma unroll
    for (int w = 0; w < 4; ++w) { mw[w] = ml[w * 8 + (r >> 2)]; lw[w] = ml[w * 8 + 4 + (r >> 2)]; }
    const unsigned m24 = f24(fmaxf(fmaxf(mw[0], mw[1]), fmaxf(mw[2], mw[3])) * SC2);
    const float Mr = __uint_as_float(m24 << 8);
    float Ls = 0.f;
#pragma unroll
    for (int w = 0; w < 4; ++w) Ls = fmaf(__builtin_amdgcn_exp2f(fmaf(mw[w], SC2, -Mr)), lw[w], Ls);
    fown = __builtin_amdgcn_exp2f(fmaf(mrun, SC2, -Mr)) / Ls;
    if (c == 0 && (lane & 51) == 0)
      st16(rs, (unsigned)W_PG + (unsigned)(kap * 4 + (r >> 2)) * PGB + (unsigned)(16384 + jg * 16), v4u{m24 | TAG_LO, TAG_HI, f24(Ls) | TAG_LO, TAG_HI});
  }
  f32x4 acc[8];
#pragma unroll
  for (int m = 0; m < 8; ++m) acc[m] = f32x4{0.f, 0.f, 0.f, 0.f};
  f32x4 basea = {0.f, 0.f, 0.f, 0.f}, baseb = {0.f, 0.f, 0.f, 0.f};
  float mcur = mq[0];
#pragma unroll 1
  for (int lap = 0; lap < NTILE / 4; ++lap) {
    if (__any(mq[0] > mcur)) {
      const float al = __builtin_amdgcn_exp2f((mcur - mq[0]) * SC2);
#pragma unroll
      for (int m = 0; m < 8; ++m) acc[m] *= al;
      mcur = mq[0];
    }
    if (c == 3 && lap == NTILE / 4 - 1) {
#pragma unroll
      for (int w = 0; w < 3; ++w) {
        lds_wait_ge(CT + C_MRG + w, 1u);
        const LAS float* rw = (const LAS float*)(L + L_RING + w * SLOT);
        basea += *(const LAS f32x4*)(rw + r * 32 + 4 * g); baseb += *(const LAS f32x4*)(rw + r * 32 + 16 + 4 * g);
      }
    }
    lds_wait_ge(CT + C_LANDED, 3u + NTILE + 4u * lap + c);
    v4u vf[2][8];
    LAS unsigned char* vbase = L + L_RING + (c + 4 * (lap & 1)) * SLOT + (4 * g + (r >> 2)) * 256 + (r & 3) * 8;
#pragma unroll
    for (int ks = 0; ks < 2; ++ks)
#pragma unroll
      for (int m = 0; m < 8; ++m) {
        LAS unsigned char* p = vbase + ks * 8192 + ((m ^ vx) * 32);
        const v2u t0 = trread(p), t1 = trread(p + 4096);
        vf[ks][m] = v4u{t0.x, t0.y, t1.x, t1.y};
      }
    LDS_WAIT();
    CT[C_DONE + c] = (unsigned)(NTILE / 4 + lap + 1);
#pragma unroll
    for (int ks = 0; ks < 2; ++ks) {
      asm volatile("" : "+v"(vf[ks][0]), "+v"(vf[ks][1]), "+v"(vf[ks][2]), "+v"(vf[ks][3]) :: "memory");
      asm volatile("" : "+v"(vf[ks][4]), "+v"(vf[ks][5]), "+v"(vf[ks][6]), "+v"(vf[ks][7]) :: "memory");
    }
#pragma unroll
    for (int ks = 0; ks < 2; ++ks)
#pragma unroll
      for (int m = 0; m < 8; ++m) acc[m] = mfma(vf[ks][m], pq[0][ks], acc[m]);
#pragma unroll
    for (int i = 0; i < 7; ++i) { pq[i][0] = pq[i + 1][0]; pq[i][1] = pq[i + 1][1]; mq[i] = mq[i + 1]; }
  }
  TS(4);
  if (c == 3) {
    const int u = r & 3;
    const f32x4 sa = u == 0 ? acc[0] : u == 1 ? acc[2] : u == 2 ? acc[4] : acc[6], sb = u == 0 ? acc[1] : u == 1 ? acc[3] : u == 2 ? acc[5] : acc[7];
    const f32x4 xa = fown * sa + basea, xb = fown * sb + baseb;
    const unsigned po = (unsigned)W_PG + (unsigned)(kap * 4 + (r >> 2)) * PGB + (unsigned)(jg * 512 + g * 64 + u * 16);
    const v2u pa = {cvtpk(xa[0], xa[1]), cvtpk(xa[2], xa[3])}, pb = {cvtpk(xb[0], xb[1]), cvtpk(xb[2], xb[3])};
    st16(rs, po, v4u{pa.x, T32, pa.y, T32});
    st16(rs, po + 256u, v4u{pb.x, T32, pb.y, T32});
    if ((r >> 2) == hh) {
      LAS unsigned char* own = L + L_RING + (jg >> 3) * SLOT + 4096 + (jg & 7) * STG_ROW + 64 * u + 8 * g;
      *(LAS v2u*)own = pa; *(LAS v2u*)(own + 32) = pb;
    }
    CT[C_SEEN] = 1u;
    TS(2);
  } else {
    LAS float* rec = (LAS float*)(L + L_RING + c * SLOT);
    if ((r & 3) == 0) {
#pragma unroll
      for (int m = 0; m < 8; ++m) *(LAS f32x4*)(rec + (r >> 2) * 128 + 16 * m + 4 * g) = fown * acc[m];
    }
    LDS_WAIT();
    CT[C_MRG + c] = 1u;
  }
  lds_wait_ge(CT + C_WGT, 1u);
  {
    LAS unsigned* stw = (LAS unsigned*)(L + L_RING + c * SLOT + 4096) + 16 * ((lane >> 1) & 3) + 8 * (lane >> 5) + 2 * ((lane >> 3) & 3) + (lane & 1);
    gu64* pgp = (gu64*)(a.ws + W_PG + (size_t)ht * PGB + c * 4096);
    gu64* dmy = ws64 + W_QG / 8 + (size_t)bid * 256;
    unsigned need = c == (jg >> 3) ? 0xffu & ~(1u << (jg & 7)) : 0xffu, ca = 0u, cb = 0u;
    u64 va[8], vb[8];
#define H1_ISSUE(v, cm) do { cm = need; \
      _Pragma("unroll") for (int i = 0; i < 8; ++i) v[i] = __hip_atomic_load(((cm >> i) & 1u) ? pgp + i * 64 + lane : dmy, RLX_AGENT); } while (0)
#define H1_CHECK(v, cm) do { _Pragma("unroll") for (int i = 0; i < 8; ++i) \
      if (((cm & need) >> i) & 1u) { if (__all((unsigned)(v[i] >> 32) == T32)) { stw[i * (STG_ROW / 4)] = (unsigned)v[i]; need &= ~(1u << i); } } } while (0)
    v4u bw[4]; f32x4 dff = {0.f, 0.f, 0.f, 0.f};
    bool pre = false;
#define PRE_WORK() do { \
      LAS unsigned char* wb = L + L_A + (8 * g + (r >> 2)) * 128 + c * 32 + (r & 3) * 8; \
      _Pragma("unroll") for (int ks = 0; ks < 4; ++ks) { const v2u t0 = trread(wb + ks * 4096), t1 = trread(wb + ks * 4096 + 512); bw[ks] = v4u{t0.x, t0.y, t1.x, t1.y}; } \
      _Pragma("unroll") for (int ks = 0; ks < 2; ++ks) { \
        const v2u t0 = trread(wb + SLOT + ks * 4096), t1 = trread(wb + SLOT + ks * 4096 + 512); \
        const v4u hf = *(const LAS v4u*)(L + L_HB + (4 * (r & 1) + (r >> 2)) * HB_ROW + 64 * ks + 16 * g); \
        dff = mfma(hf, v4u{t0.x, t0.y, t1.x, t1.y}, dff); \
      } \
      asm volatile("" : "+v"(bw[0]), "+v"(bw[1]), "+v"(bw[2]), "+v"(bw[3]), "+v"(dff) :: "memory"); } while (0)
    lds_wait_ge(CT + C_SEEN, 1u);
    {
      const unsigned tgo = CT[C_TGO];
#pragma unroll 1
      for (int sp = 0; sp < 256 && (int)((unsigned)__builtin_amdgcn_s_memrealtime() - tgo) < 0; ++sp) __builtin_amdgcn_s_sleep(1);
    }
    H1_ISSUE(va, ca);
    _Pragma("unroll 1") for (unsigned sp = 0; sp < GSPIN; ++sp) {
      if (__builtin_popcount(need) <= H1_DIRECT) __builtin_amdgcn_s_sleep(H1_SLEEP); else __builtin_amdgcn_s_sleep(ML_SLEEP);
      H1_ISSUE(vb, cb);
      if (!pre && CT[C_WOK] != 0u) { CBAR(); PRE_WORK(); pre = true; }
      H1_CHECK(va, ca);
      if (need == 0u) break;
      if (__builtin_popcount(need) <= H1_DIRECT) __builtin_amdgcn_s_sleep(H1_SLEEP); else __builtin_amdgcn_s_sleep(ML_SLEEP);
      H1_ISSUE(va, ca);
      H1_CHECK(vb, cb);
      if (need == 0u) break;
    }
    TS(6);
    LDS_WAIT();
    CT[C_STG + c] = 1u;
    if (!pre) { lds_wait_ge(CT + C_WOK, 1u); PRE_WORK(); }
    TS(5);
#pragma unroll 1
    for (unsigned sp = 0; sp < LSPIN; ++sp) { const v4u fl = *(volatile LAS v4u*)(CT + C_STG); if (fl.x & fl.y & fl.z & fl.w) break; }
    CBAR();
    if (c == 3) TS(3);
    const LAS float* mls = (const LAS float*)(L + L_ML);
    LAS unsigned char* stg = L + L_RING + (r >> 3) * SLOT + 4096 + (r & 7) * STG_ROW + 16 * g;
    unsigned y[2];
#pragma unroll
    for (int rt = 0; rt < 2; ++rt) {
      f32x4 d = {0.f, 0.f, 0.f, 0.f};
#pragma unroll
      for (int ks = 0; ks < 4; ++ks) d = mfma(*(const LAS v4u*)(stg + 2 * rt * SLOT + 64 * ks), bw[ks], d);
      const f32x4 w = *(const LAS f32x4*)(mls + 16 * rt + 4 * g);
      y[rt] = f24(fmaf(1.f - __uint_as_float(CT[C_G + 4 * rt + g]), dff[rt], (w[0] * d[0] + w[1] * d[1]) + (w[2] * d[2] + w[3] * d[3]))) | TAG_LO;
    }
    const unsigned snd = (r & 1) ? y[0] : y[1];
    const unsigned rcv = (unsigned)__builtin_amdgcn_mov_dpp((int)snd, 0xB1, 0xf, 0xf, true);
    st16(rs, (unsigned)W_OG + (unsigned)((((4 * (r & 1) + g) * 32 + 4 * n8 + c) * 32 + ht) * 16 + (r & 14)) * 8u,
         (r & 1) ? v4u{rcv, TAG_HI, y[1], TAG_HI} : v4u{y[0], TAG_HI, rcv, TAG_HI});
  }
  if (c == 0) TS(2);
  TS_FLUSH(wave);
}
}

extern "C" void kernel_launch(void* const* d_in, const int* in_sizes, int n_in, void* d_out, int out_size, void* d_ws,
                              size_t ws_size, hipStream_t stream) {
  (void)in_sizes; (void)n_in; (void)out_size;
  if (ws_size < W_END) return;
  Args a;
  a.x = (const unsigned char*)d_in[0]; a.k = (const unsigned char*)d_in[1]; a.v = (const unsigned char*)d_in[2];
  a.wq = (const unsigned char*)d_in[3]; a.wo = (const unsigned char*)d_in[4];
  a.wg = (const unsigned char*)d_in[6]; a.wu = (const unsigned char*)d_in[7]; a.wd = (const unsigned char*)d_in[8];
  a.gw = (const unsigned char*)d_in[9];
  a.out = (unsigned char*)d_out; a.ws = (unsigned char*)d_ws;
  (void)hipFuncSetAttribute((const void*)fused_kernel, hipFuncAttributeMaxDynamicSharedMemorySize, L_BYTES);
  hipLaunchKernelGGL(fused_kernel, dim3(NWG), dim3(NTHR), L_BYTES, stream, a);
}
```

```cpp
#include <hip/hip_runtime.h>
#include <hip/hip_bf16.h>
#include <stdint.h>

#define GAS __attribute__((address_space(1)))
#define LAS __attribute__((address_space(3)))
typedef unsigned v4u __attribute__((ext_vector_type(4)));
typedef unsigned v2u __attribute__((ext_vector_type(2)));
typedef float f32x4 __attribute__((ext_vector_type(4)));
typedef float f32x2 __attribute__((ext_vector_type(2)));
typedef __bf16 bf16x8 __attribute__((ext_vector_type(8)));
typedef short s16x4 __attribute__((ext_vector_type(4)));
typedef unsigned long long u64;
typedef GAS u64 gu64;
#define RLX_AGENT __ATOMIC_RELAXED, __HIP_MEMORY_SCOPE_AGENT

namespace {
constexpr int NB = 8, DM = 512, HQ = 32, HKV = 8, SK = 8192, DH = 128, FF = 1792;
constexpr int NWG = 256, NTHR = 6 * 64;
constexpr int NTILE = 32;
constexpr int PACE_R = 550, PACE_0 = 46, PACE_D = 44, H1_GO = 170;
constexpr int H1_DIRECT = 4;
constexpr int SLOT = 16384;
constexpr int L_RING = 0, L_A = 8 * SLOT, L_B = L_A + SLOT, L_Q = L_B + 8192, L_HB = L_Q + 1024, L_ML = L_HB + 2048, L_WML = L_ML + 128, L_CTRL = L_ML + 256, L_BYTES = L_CTRL + 256;
constexpr int STG_ROW = 272;
constexpr int HB_ROW = 144;
constexpr int C_LANDED = 0, C_GUFREE = 1, C_QRDY = 2, C_PPUB = 3, C_DONE = 4, C_MRG = 8, C_STG = 12, C_TAG = 16, C_TAGOK = 18, C_WOK = 19, C_LAG = 21, C_PB = 22, C_PR = 23, C_G = 24, C_P0 = 32, C_TGO = 33, C_WGT = 34, C_KML = 36;
static_assert(L_BYTES <= 160 * 1024, "LDS");
constexpr size_t W_EPOCH = 0, W_G = 256, W_FLAG = 4096, W_LAG = 8192;
constexpr size_t W_QG = 16384;
constexpr size_t W_GP = W_QG + (size_t)2 * NB * 4096 * 8;
constexpr size_t W_HG = W_GP + (size_t)224 * 2048;
constexpr size_t W_PG = W_HG + (size_t)NB * FF * 8;
constexpr unsigned PGB = 16384 + 512;
constexpr size_t W_OG = W_PG + (size_t)HQ * PGB;
constexpr size_t W_END = W_OG + (size_t)NWG * 4096;
static_assert(W_END < (1u << 30) && W_PG % 1024 == 0, "buffer offsets are int");
constexpr u64 EPOCH_STEP = 0x9E3779B97F4A7C15ull;
constexpr unsigned GSPIN = 1u << 15, LSPIN = 1u << 18;
constexpr float SC2 = 0.08838834764831845f * 1.4426950408889634f;

struct Args {
  const unsigned char *x, *k, *v, *wq, *wo, *wg, *wu, *wd, *gw;
  unsigned char *out, *ws;
};

__device__ __forceinline__ f32x4 mfma(v4u a, v4u b, f32x4 c) {
  return __builtin_amdgcn_mfma_f32_16x16x32_bf16(__builtin_bit_cast(bf16x8, a), __builtin_bit_cast(bf16x8, b), c, 0, 0, 0);
}
__device__ __forceinline__ v2u trread(LAS unsigned char* p) {
  return __builtin_bit_cast(v2u, __builtin_amdgcn_ds_read_tr16_b64_v4i16((LAS s16x4*)p));
}
__device__ __forceinline__ unsigned cvtpk(float lo, float hi) {
  typedef __bf16 bf16x2 __attribute__((ext_vector_type(2)));
  bf16x2 r; r[0] = (__bf16)lo; r[1] = (__bf16)hi; return __builtin_bit_cast(unsigned, r);
}
template <int CTRL> __device__ __forceinline__ float dpp(float x) {
  return __builtin_bit_cast(float, __builtin_amdgcn_mov_dpp(__builtin_bit_cast(int, x), CTRL, 0xf, 0xf, true));
}
__device__ __forceinline__ float xrow_max(float x) {
  auto s = __builtin_amdgcn_permlane16_swap(__float_as_uint(x), __float_as_uint(x), false, false);
  x = fmaxf(__uint_as_float(s[0]), __uint_as_float(s[1]));
  auto t = __builtin_amdgcn_permlane32_swap(__float_as_uint(x), __float_as_uint(x), false, false);
  return fmaxf(__uint_as_float(t[0]), __uint_as_float(t[1]));
}
__device__ __forceinline__ float xrow_sum(float x) {
  auto s = __builtin_amdgcn_permlane16_swap(__float_as_uint(x), __float_as_uint(x), false, false);
  x = __uint_as_float(s[0]) + __uint_as_float(s[1]);
  auto t = __builtin_amdgcn_permlane32_swap(__float_as_uint(x), __float_as_uint(x), false, false);
  return __uint_as_float(t[0]) + __uint_as_float(t[1]);
}
__device__ __forceinline__ float bflo(unsigned u) { return __uint_as_float(u << 16); }
__device__ __forceinline__ float bfhi(unsigned u) { return __uint_as_float(u & 0xffff0000u); }

__device__ __forceinline__ v4u ld16(__amdgpu_buffer_rsrc_t rs, unsigned off) { return __builtin_amdgcn_raw_buffer_load_b128(rs, (int)off, 0, 16); }
__device__ __forceinline__ void st16(__amdgpu_buffer_rsrc_t rs, unsigned off, v4u v) { __builtin_amdgcn_raw_buffer_store_b128(v, rs, (int)off, 0, 16); }
__device__ __forceinline__ unsigned f24(float v) { return (__float_as_uint(v) + 0x80u) >> 8; }
#define TAG_LO ((unsigned)T64 << 24)
#define TAG_HI ((unsigned)(T64 >> 8))
#define TAG_OK(lo, hi) ((hi) == TAG_HI && ((lo) ^ TAG_LO) < 0x1000000u)
#define CBAR() asm volatile("" ::: "memory")
#define LDS_WAIT() asm volatile("s_waitcnt lgkmcnt(0)" ::: "memory")
#define DMA4(sbase, dst, v0, v1, v2, v3)                                                                         \
  asm volatile("s_mov_b32 m0, %5\n\ts_nop 0\n\t"                                                                 \
               "global_load_lds_dwordx4 %0, %4 nt\n\t"                                                           \
               "global_load_lds_dwordx4 %1, %4 offset:1024 nt\n\t"                                               \
               "global_load_lds_dwordx4 %2, %4 offset:2048 nt\n\t"                                               \
               "global_load_lds_dwordx4 %3, %4 offset:3072 nt"                                                   \
               :: "v"(v0), "v"(v1), "v"(v2), "v"(v3), "s"(sbase), "s"(dst) : "memory")
#define DMA1S(sbase, voff, dst)                                                                                  \
  asm volatile("s_mov_b32 m0, %2\n\ts_nop 0\n\tglobal_load_lds_dwordx4 %0, %1 nt" :: "v"(voff), "s"(sbase), "s"(dst) : "memory")
#define DMA1V(vaddr, dst)                                                                                        \
  asm volatile("s_mov_b32 m0, %1\n\ts_nop 0\n\tglobal_load_lds_dwordx4 %0, off nt" :: "v"(vaddr), "s"(dst) : "memory")
#define VMWAIT(n) asm volatile("s_waitcnt vmcnt(" #n ")" ::: "memory")

__device__ __forceinline__ void lds_wait_ge(volatile LAS unsigned* w, unsigned need) {
#pragma unroll 1
  for (unsigned sp = 0; *w < need && sp < LSPIN; ++sp) __builtin_amdgcn_s_sleep(1);
  CBAR();
}

__global__ void __launch_bounds__(NTHR, 2) fused_kernel(Args a) {
  extern __shared__ __attribute__((aligned(16))) unsigned char smem[];
  LAS unsigned char* L = (LAS unsigned char*)smem;
  volatile LAS unsigned* CT = (volatile LAS unsigned*)(L + L_CTRL);
  const int tid = threadIdx.x, lane = tid & 63, wave = __builtin_amdgcn_readfirstlane(tid >> 6);
  const int bid = blockIdx.x;
  const int kap = bid >> 5, jg = bid & 31, b = jg >> 2, s = jg & 3;
  const int qcol = kap * 512 + (((jg & 7) << 1) | ((jg >> 3) & 1)) * 32, qk = jg >> 4;
  const bool has_ffn = (bid >> 3) < 28;
  const int fi = has_ffn ? (bid >> 3) : 27;
  const int fcg = (bid & 7) * 7 + (fi >> 2), fk = fi & 3;
  const int hh = jg >> 3, ht = kap * 4 + hh, n8 = jg & 7;
  const int g = lane >> 4, r = lane & 15;
  gu64* ws64 = (gu64*)a.ws;
  const __amdgpu_buffer_rsrc_t rs = __builtin_amdgcn_make_buffer_rsrc((void*)a.ws, 0, (int)W_END, 0x00020000);
  if (__builtin_expect(wave == 0, 1)) {
    __builtin_amdgcn_s_waitcnt(0);
    __builtin_amdgcn_s_setprio(3);
    const unsigned lb = (unsigned)(size_t)L;
    {
      const unsigned voff = (unsigned)(lane >> 2) * 8192u + (unsigned)(lane & 3) * 16u;
      const unsigned char* sb = a.wq + (size_t)qk * 256 * 8192 + (size_t)qcol * 2;
#pragma unroll 1
      for (int j = 0; j < 16; ++j) DMA1S(sb + (size_t)j * 16 * 8192, voff, lb + L_A + j * 1024);
    }
    {
      const unsigned char* p = ((lane & 32) ? a.wu : a.wg) + (size_t)(fk * 128 + ((lane >> 2) & 7)) * (FF * 2) + (size_t)fcg * 64 + (lane & 3) * 16;
#pragma unroll 1
      for (int j = 0; j < 16; ++j) DMA1V(p + (size_t)j * 8 * (FF * 2), lb + L_RING + 7 * SLOT + j * 1024);
    }
    __builtin_amdgcn_s_barrier();
    CBAR();
    unsigned ko[4], vo[2];
#pragma unroll
    for (int j = 0; j < 4; ++j) ko[j] = (unsigned)g * 256u + (unsigned)((r ^ g ^ (4 * j)) * 16);
#pragma unroll
    for (int j = 0; j < 2; ++j) vo[j] = (unsigned)g * 256u + (unsigned)((r ^ (2 * (4 * j + g))) * 16);
    const size_t seq_off = (size_t)(b * HKV + kap) * SK * 256;
    const unsigned char* kb = a.k + seq_off;
    const unsigned char* vb = a.v + seq_off;
    unsigned seen = 0;
    unsigned pb = (unsigned)__builtin_amdgcn_s_memrealtime() + (unsigned)PACE_0;
    int pr = PACE_R;
    CT[C_P0] = pb;
    bool deep = true;
#pragma unroll 1
    for (int hv = 0; hv < 2; ++hv) {
      const unsigned o0 = hv ? vo[0] : ko[0], o1 = hv ? vo[1] : ko[1], o2 = hv ? vo[0] : ko[2], o3 = hv ? vo[1] : ko[3];
      const unsigned char* xb = hv ? vb : kb;
#pragma unroll 1
      for (int n = 0; n < NTILE; ++n) {
        const int tile = ((n + 4 * b) & 31) * 4 + s;
        const int m = hv * NTILE + n;
        const unsigned f = 2u + m;
        if (m >= 7) {
          volatile LAS unsigned* fw = m >= 8 ? (CT + C_DONE + (m & 3)) : (CT + C_GUFREE);
          const unsigned need = m >= 8 ? (unsigned)((m >> 2) - 1) : 1u;
          if (m < 8 || seen < need) {
            if (*fw < need) {
              VMWAIT(0); CT[C_LANDED] = f;
              lds_wait_ge(fw, need);
            }
          }
        }
        const unsigned char* sb = xb + (size_t)tile * SLOT;
        const unsigned dst = lb + L_RING + (m & 7) * SLOT;
        DMA4(sb, dst, o0, o1, o2, o3); DMA4(sb + 4096, dst + 4096, o0, o1, o2, o3);
        if (deep) VMWAIT(32); else VMWAIT(16);
        DMA4(sb + 8192, dst + 8192, o0, o1, o2, o3); DMA4(sb + 12288, dst + 12288, o0, o1, o2, o3);
        const unsigned nx = CT[C_DONE + ((m + 1) & 3)];
        const unsigned now = (unsigned)__builtin_amdgcn_s_memrealtime();
        const v2u ps = *(volatile LAS v2u*)(CT + C_PB);
        if (deep) { VMWAIT(32); CT[C_LANDED] = f - 1; } else { VMWAIT(16); CT[C_LANDED] = f; }
        CT[C_LAG] = now - (unsigned)(PACE_R * (int)f / 10);
        if (__builtin_amdgcn_readfirstlane(ps.y)) { pb = __builtin_amdgcn_readfirstlane(ps.x); pr = __builtin_amdgcn_readfirstlane(ps.y); }
        deep = ((int)(now - pb - (unsigned)(pr * (int)f / 10)) > PACE_D && f < 2u * NTILE) || m == 0;
        seen = __builtin_amdgcn_readfirstlane(nx);
      }
    }
    CT[C_TGO] = pb + (unsigned)(pr * (2 * NTILE + 1) / 10 + H1_GO);
    VMWAIT(16); CT[C_LANDED] = 1u + 2u * NTILE;
    {
      const unsigned voff = (unsigned)(lane >> 3) * 1024u + (unsigned)(lane & 7) * 16u;
      const unsigned char* sb = a.wo + (size_t)ht * 128 * 1024 + (size_t)n8 * 128;
      const unsigned char* sd = a.wd + (size_t)ht * 56 * 1024 + (size_t)n8 * 128;
#pragma unroll 1
      for (int j = 0; j < 23; ++j) {
        DMA1S(j < 16 ? sb + (size_t)j * 8192 : sd + (size_t)(j - 16) * 8192, voff, lb + L_A + j * 1024);
        if (j == 7) { VMWAIT(8); CT[C_LANDED] = 2u + 2u * NTILE; }
      }
      VMWAIT(0);
      CT[C_WOK] = 1u;
    }
    return;
  }

  const int c = wave - 1;
  u64 T64 = 0;
  v4u xf[8];
  if (wave == 5) T64 = __hip_atomic_load(ws64 + W_EPOCH / 8, RLX_AGENT);
  if (wave <= 2) {
    const GAS v4u* xp = (const GAS v4u*)(a.x + (size_t)(r & 7) * 1024 + (wave == 1 ? qk * 512 : fk * 256) + g * 16);
#pragma unroll
    for (int ks = 0; ks < 8; ++ks) xf[ks] = xp[(wave == 1 ? ks : (ks & 3)) * 4];
  }
  if (wave == 3) ((LAS unsigned*)(L + L_CTRL))[lane] = 0u;
  if (wave == 4) ((LAS v4u*)(L + L_B + 7168))[lane] = v4u{0u, 0u, 0u, 0u};
  LDS_WAIT();
  __builtin_amdgcn_s_barrier();
  CBAR();

  if (wave == 5) {
    T64 += EPOCH_STEP;
    if (lane == 0) { CT[C_TAG] = (unsigned)T64; CT[C_TAG + 1] = (unsigned)(T64 >> 32); LDS_WAIT(); CT[C_TAGOK] = 1u; }
#define PACE_POST(k) do { if (lane == 0) __hip_atomic_store(ws64 + (W_LAG + (k) * 2048) / 8 + bid, (T64 << 32) | CT[C_LAG], RLX_AGENT); } while (0)
#define PACE_MEAN(k, m, ok) do { const unsigned p0 = CT[C_P0]; \
      const u64 lv = __hip_atomic_load(ws64 + (W_LAG + (k) * 2048) / 8 + kap * 32 + (lane & 31), RLX_AGENT); \
      const bool vk = (unsigned)(lv >> 32) == (unsigned)T64 && lane < 32; \
      int dl = vk ? (int)((unsigned)lv - p0) : 0; dl = dl > 2000 ? 2000 : dl < -2000 ? -2000 : dl; \
      float sm = (float)dl; sm += dpp<0xB1>(sm); sm += dpp<0x4E>(sm); sm += dpp<0x141>(sm); sm += dpp<0x140>(sm); sm = xrow_sum(sm); \
      const int cn = __builtin_popcountll(__ballot(vk)); ok = cn >= 24; m = sm / (float)(cn > 0 ? cn : 1); } while (0)
#define PACE_SET(m, f, dr) do { int nr = PACE_R + (int)(dr); nr = nr < 520 ? 520 : nr > 680 ? 680 : nr; \
      if (lane == 0) *(volatile LAS v2u*)(CT + C_PB) = v2u{CT[C_P0] + (unsigned)(int)(m) - (unsigned)((nr - PACE_R) * (f) / 10), (unsigned)nr}; } while (0)
    float m0; bool ok0;
    if (bid == NWG - 1) {
      const int gb = lane >> 3, part = lane & 7;
      const GAS v4u* xp = (const GAS v4u*)(a.x + (size_t)gb * 1024 + part * 128);
      const GAS v4u* gp = (const GAS v4u*)(a.gw + part * 128);
      float ga = 0.f;
#pragma unroll
      for (int i = 0; i < 8; ++i) {
        const v4u xv = xp[i], gv = gp[i];
#pragma unroll
        for (int e = 0; e < 4; ++e) { ga = fmaf(bflo(xv[e]), bflo(gv[e]), ga); ga = fmaf(bfhi(xv[e]), bfhi(gv[e]), ga); }
      }
      ga += dpp<0xB1>(ga); ga += dpp<0x4E>(ga); ga += dpp<0x141>(ga);
      const float gate = 1.f / (1.f + __expf(-ga));
      if (part == 0) __hip_atomic_store(ws64 + W_G / 8 + gb, ((u64)TAG_HI << 32) | (f24(gate) | TAG_LO), RLX_AGENT);
    }
    {
      lds_wait_ge(CT + C_LANDED, 6u);
      const unsigned qo = (unsigned)W_QG + (unsigned)((b * HKV + kap) * 8192 + lane * 16);
      _Pragma("unroll 1") for (unsigned sp = 0; sp < GSPIN; ++sp) {
        v4u v[8]; bool ok = true;
#pragma unroll
        for (int i = 0; i < 8; ++i) v[i] = ld16(rs, qo + i * 1024u);
#pragma unroll
        for (int i = 0; i < 4; ++i) {
          ok &= TAG_OK(v[i].x, v[i].y) && TAG_OK(v[i].z, v[i].w) && TAG_OK(v[i + 4].x, v[i + 4].y) && TAG_OK(v[i + 4].z, v[i + 4].w);
          ((LAS unsigned*)(L + L_Q))[i * 64 + lane] = cvtpk(__uint_as_float(v[i].x << 8) + __uint_as_float(v[i + 4].x << 8), __uint_as_float(v[i].z << 8) + __uint_as_float(v[i + 4].z << 8));
        }
        if (__all(ok)) break;
        __builtin_amdgcn_s_sleep(8);
      }
      LDS_WAIT(); CT[C_QRDY] = 1u;
    }
    {
      lds_wait_ge(CT + C_LANDED, 12u);
      PACE_POST(0);
      const int fb = lane < 32 ? kap * 32 + lane : (bid & 7) + 8 * ((fi & ~3) + (lane & 3));
      _Pragma("unroll 1") for (unsigned sp = 0; sp < GSPIN; ++sp) {
        if (__all(__hip_atomic_load(ws64 + W_FLAG / 8 + fb, RLX_AGENT) == T64)) break;
        __builtin_amdgcn_s_sleep(32);
      }
      CBAR();
    }
    if (has_ffn) {
      const unsigned go = (unsigned)W_GP + (unsigned)((fcg * 4 + fk) * 2048 + lane * 16);
      const f32x4 p0 = __builtin_bit_cast(f32x4, ld16(rs, go)), p1 = __builtin_bit_cast(f32x4, ld16(rs, go + 1024u));
      unsigned hw[4];
#pragma unroll
      for (int e = 0; e < 4; ++e) {
        const unsigned t = __float_as_uint(p0[e] + p1[e]);
        auto s32 = __builtin_amdgcn_permlane32_swap(t, t, false, false);
        const unsigned tot = __float_as_uint(__uint_as_float(s32[0]) + __uint_as_float(s32[1]));
        auto s16 = __builtin_amdgcn_permlane16_swap(tot, tot, false, false);
        const float gt = __uint_as_float(s16[0]), up = __uint_as_float(s16[1]);
        const float u = 0.7978845608028654f * (gt + 0.044715f * gt * gt * gt);
        const float th = 1.f - 2.f / (__expf(2.f * u) + 1.f);
        hw[e] = f24(0.5f * gt * (1.f + th) * up) | TAG_LO;
      }
      const int src = ((2 * (g & 1) + (r >> 3)) * 4 + ((r & 7) >> 1)) * 4;
      const unsigned a0 = __builtin_amdgcn_ds_bpermute(src, (int)hw[0]), a1 = __builtin_amdgcn_ds_bpermute(src, (int)hw[1]);
      const unsigned a2 = __builtin_amdgcn_ds_bpermute(src, (int)hw[2]), a3 = __builtin_amdgcn_ds_bpermute(src, (int)hw[3]);
      if (lane < 32)
        st16(rs, (unsigned)W_HG + (unsigned)((4 * (fk & 1) + 2 * g + (r >> 3)) * FF + fcg * 32 + (fk >> 1) * 16 + 2 * (r & 7)) * 8u,
             v4u{(r & 1) ? a2 : a0, TAG_HI, (r & 1) ? a3 : a1, TAG_HI});
    }
    {
      lds_wait_ge(CT + C_LANDED, 20u);
      PACE_MEAN(0, m0, ok0);
      if (ok0) PACE_SET(m0, 12, m0 * 0.5f);
      const bool pad = (lane & 31) >= 28;
      const unsigned ho = (unsigned)W_HG + (unsigned)((lane >> 5) * FF + ht * 56 + (pad ? 0 : 2 * (lane & 31))) * 8u;
      _Pragma("unroll 1") for (unsigned sp = 0; sp < GSPIN; ++sp) {
        v4u v[4];
#pragma unroll
        for (int i = 0; i < 4; ++i) v[i] = ld16(rs, ho + (unsigned)(i * 2 * FF * 8));
        const u64 gv = __hip_atomic_load(ws64 + W_G / 8 + (lane & 7), RLX_AGENT);
        bool ok = TAG_OK((unsigned)gv, (unsigned)(gv >> 32));
#pragma unroll
        for (int i = 0; i < 4; ++i) {
          ok &= TAG_OK(v[i].x, v[i].y) && TAG_OK(v[i].z, v[i].w);
          *(LAS unsigned*)(L + L_HB + (2 * i + (lane >> 5)) * HB_ROW + (lane & 31) * 4) =
              pad ? 0u : cvtpk(__uint_as_float(v[i].x << 8), __uint_as_float(v[i].z << 8));
        }
        if (lane < 8) CT[C_G + lane] = (unsigned)gv << 8;
        if (__all(ok)) break;
        __builtin_amdgcn_s_sleep(32);
      }
      LDS_WAIT();
    }
    {
      float m1, m2; bool ok1, ok2;
      lds_wait_ge(CT + C_LANDED, 27u);
      PACE_POST(1);
      lds_wait_ge(CT + C_LANDED, 34u);
      PACE_MEAN(1, m1, ok1);
      if (ok0 && ok1) PACE_SET(m1, 27, (m1 - m0) * (10.f / 15.f));
      lds_wait_ge(CT + C_LANDED, 43u);
      PACE_POST(2);
      lds_wait_ge(CT + C_LANDED, 50u);
      PACE_MEAN(2, m2, ok2);
      if (ok1 && ok2) PACE_SET(m2, 43, (m2 - m1) * (10.f / 16.f));
    }
    {
      const unsigned mo = (unsigned)W_PG + (unsigned)ht * PGB + (unsigned)(16384 + (lane & 31) * 16);
      v4u mv;
      _Pragma("unroll 1") for (unsigned sp = 0; sp < GSPIN; ++sp) {
        mv = ld16(rs, mo);
        if (__all(TAG_OK(mv.x, mv.y) && TAG_OK(mv.z, mv.w))) break;
        __builtin_amdgcn_s_sleep(32);
      }
      const float ms = __uint_as_float(mv.x << 8);
      float mx = fmaxf(ms, dpp<0xB1>(ms)); mx = fmaxf(mx, dpp<0x4E>(mx));
      const float wl = __builtin_amdgcn_exp2f(ms - mx) * __uint_as_float(mv.z << 8);
      float lt = wl + dpp<0xB1>(wl); lt += dpp<0x4E>(lt);
      if (lane < 32) ((LAS float*)(L + L_ML))[lane] = __uint_as_float(CT[C_G + (lane >> 2)]) * wl / lt;
      LDS_WAIT();
      CT[C_WGT] = 1u;
    }
    {
      const unsigned oo = (unsigned)W_OG + (unsigned)(bid * 4096), ol = (unsigned)lane * 16u;
      float t0[4], t1[4];
      v4u va[4], vb[4];
      unsigned need = 15u, ca, cb;
#define H2_ISSUE(v, cm) do { cm = need; _Pragma("unroll") for (int i = 0; i < 4; ++i) v[i] = ld16(rs, ((cm >> i) & 1u) ? oo + i * 1024u + ol : (unsigned)W_QG + (unsigned)bid * 2048u); } while (0)
#define H2_CHECK(v, cm) do { _Pragma("unroll") for (int i = 0; i < 4; ++i) if (((cm & need) >> i) & 1u) { \
        if (__all(TAG_OK(v[i].x, v[i].y) && TAG_OK(v[i].z, v[i].w))) { t0[i] = __uint_as_float(v[i].x << 8); t1[i] = __uint_as_float(v[i].z << 8); need &= ~(1u << i); } } } while (0)
#pragma unroll 1
      for (unsigned sp = 0; sp < LSPIN; ++sp) { const v4u fl = *(volatile LAS v4u*)(CT + C_STG); if (fl.x & fl.y & fl.z & fl.w) break; __builtin_amdgcn_s_sleep(1); }
      H2_ISSUE(va, ca);
      _Pragma("unroll 1") for (unsigned sp = 0; sp < GSPIN; ++sp) {
        __builtin_amdgcn_s_sleep(1);
        H2_ISSUE(vb, cb);
        H2_CHECK(va, ca);
        if (need == 0u) break;
        __builtin_amdgcn_s_sleep(1);
        H2_ISSUE(va, ca);
        H2_CHECK(vb, cb);
        if (need == 0u) break;
      }
      float s0 = (t0[0] + t0[1]) + (t0[2] + t0[3]), s1 = (t1[0] + t1[1]) + (t1[2] + t1[3]);
      s0 += dpp<0x128>(s0); s1 += dpp<0x128>(s1);
      s0 = xrow_sum(s0); s1 = xrow_sum(s1);
      if (lane < 8) ((GAS unsigned*)a.out)[bid * 8 + lane] = cvtpk(s0, s1);
      if (bid == 0 && lane == 0) __hip_atomic_store(ws64 + W_EPOCH / 8, T64, RLX_AGENT);
    }
    return;
  }

  if (c == 0) {
    lds_wait_ge(CT + C_TAGOK, 1u);
    T64 = ((u64)CT[C_TAG + 1] << 32) | CT[C_TAG];
    lds_wait_ge(CT + C_LANDED, 1u);
    LAS unsigned char* sl = L + L_A + (8 * g + (r >> 2)) * 64 + (r & 3) * 8;
    f32x4 d[2] = {{0.f, 0.f, 0.f, 0.f}, {0.f, 0.f, 0.f, 0.f}};
#pragma unroll
    for (int ks = 0; ks < 8; ++ks)
#pragma unroll
      for (int ct = 0; ct < 2; ++ct) {
        const v2u b0 = trread(sl + ks * 2048 + ct * 32), b1 = trread(sl + ks * 2048 + ct * 32 + 256);
        d[ct] = mfma(xf[ks], v4u{b0.x, b0.y, b1.x, b1.y}, d[ct]);
      }
    const f32x4 dv = g < 2 ? d[0] : d[1];
    gu64* qg = ws64 + W_QG / 8 + (size_t)((4 * (g & 1) * HKV + kap) * 2 + qk) * 512 + (qcol & 511) + (g >> 1) * 16 + r;
#pragma unroll
    for (int e = 0; e < 4; ++e) __hip_atomic_store(qg + (size_t)e * HKV * 1024, ((u64)TAG_HI << 32) | (f24(dv[e]) | TAG_LO), RLX_AGENT);
  } else if (c == 1) {
    lds_wait_ge(CT + C_TAGOK, 1u);
    T64 = ((u64)CT[C_TAG + 1] << 32) | CT[C_TAG];
    const v4u inv = {0u, ~(unsigned)T64, 0u, ~(unsigned)T64};
    const unsigned co = (unsigned)W_PG + (unsigned)(kap * 4 + (lane >> 5)) * PGB + (unsigned)(jg * 512 + (lane & 31) * 16);
    st16(rs, co, inv);
    st16(rs, co + 2u * PGB, inv);
    lds_wait_ge(CT + C_LANDED, 2u);
    LAS unsigned char* sl = L + L_RING + 7 * SLOT + g * 1024 + (r >> 2) * 64 + (r & 3) * 8;
    f32x4 d[4] = {{0.f, 0.f, 0.f, 0.f}, {0.f, 0.f, 0.f, 0.f}, {0.f, 0.f, 0.f, 0.f}, {0.f, 0.f, 0.f, 0.f}};
#pragma unroll
    for (int ks = 0; ks < 4; ++ks)
#pragma unroll
      for (int mc = 0; mc < 4; ++mc) {
        LAS unsigned char* q = sl + ks * 4096 + (mc >> 1) * 512 + (mc & 1) * 32;
        const v2u b0 = trread(q), b1 = trread(q + 256);
        d[mc] = mfma(xf[ks], v4u{b0.x, b0.y, b1.x, b1.y}, d[mc]);
      }
    LDS_WAIT();
    CT[C_GUFREE] = 1u;
    if (has_ffn) {
      const f32x4 dg = g < 2 ? d[0] : d[1], du = g < 2 ? d[2] : d[3];
      GAS unsigned* gp = (GAS unsigned*)(a.ws + W_GP + (size_t)(fcg * 4 + 2 * (g >> 1) + (g & 1)) * 2048 + fk * 512) + r;
#pragma unroll
      for (int e = 0; e < 4; ++e) {
        __hip_atomic_store(gp + e * 16, __float_as_uint(dg[e]), RLX_AGENT);
        __hip_atomic_store(gp + 64 + e * 16, __float_as_uint(du[e]), RLX_AGENT);
      }
    }
    VMWAIT(0);
    if (lane == 0) __hip_atomic_store(ws64 + W_FLAG / 8 + bid, T64, RLX_AGENT);
  } else {
    lds_wait_ge(CT + C_TAGOK, 1u);
    T64 = ((u64)CT[C_TAG + 1] << 32) | CT[C_TAG];
  }
  const unsigned T32 = (unsigned)T64;
  const v4u ones = {0x3F803F80u, 0x3F803F80u, 0x3F803F80u, 0x3F803F80u};
  const int vx = 4 * (g & 1) + (r >> 2);

  v4u qf[4], pq[8][2];
  float mq[8];
  f32x4 accl = {0.f, 0.f, 0.f, 0.f};
  float mrun = -INFINITY;
#pragma unroll
  for (int i = 0; i < 8; ++i) { pq[i][0] = v4u{0u, 0u, 0u, 0u}; pq[i][1] = pq[i][0]; mq[i] = 0.f; }
#define READ_K(dstk, lp) do { \
      lds_wait_ge(CT + C_LANDED, 3u + 4u * (lp) + c); \
      LAS unsigned char* kbase = L + L_RING + (c + 4 * ((lp) & 1)) * SLOT + r * 256; \
      _Pragma("unroll") for (int T = 0; T < 4; ++T) \
        _Pragma("unroll") for (int ks = 0; ks < 4; ++ks) dstk[T][ks] = *(const LAS v4u*)(kbase + T * 4096 + (((4 * ks + g) ^ r) * 16)); \
      LDS_WAIT(); \
      CT[C_DONE + c] = (unsigned)((lp) + 1); \
      _Pragma("unroll") for (int T = 0; T < 4; ++T) asm volatile("" : "+v"(dstk[T][0]), "+v"(dstk[T][1]), "+v"(dstk[T][2]), "+v"(dstk[T][3]) :: "memory"); } while (0)
  {
    v4u kf[4][4], kn[4][4];
    READ_K(kf, 0);
    READ_K(kn, 1);
    lds_wait_ge(CT + C_QRDY, 1u);
#pragma unroll
    for (int ks = 0; ks < 4; ++ks) qf[ks] = *(const LAS v4u*)(L + L_Q + ((r >> 2) * 128 + 32 * ks + 8 * g) * 2);
#pragma unroll 1
    for (int lap = 0; lap < NTILE / 4; ++lap) {
      if (lap == 1) {
#pragma unroll
        for (int T = 0; T < 4; ++T)
#pragma unroll
          for (int ks = 0; ks < 4; ++ks) kf[T][ks] = kn[T][ks];
      } else if (lap) READ_K(kf, lap);
      f32x4 sc[4];
#pragma unroll
      for (int T = 0; T < 4; ++T) {
        f32x4 d = {0.f, 0.f, 0.f, 0.f};
#pragma unroll
        for (int ks = 0; ks < 4; ++ks) d = mfma(kf[T][ks], qf[ks], d);
        sc[T] = d;
      }
      float tm = fmaxf(fmaxf(sc[0][0], sc[0][1]), fmaxf(sc[0][2], sc[0][3]));
#pragma unroll
      for (int T = 1; T < 4; ++T) tm = fmaxf(tm, fmaxf(fmaxf(sc[T][0], sc[T][1]), fmaxf(sc[T][2], sc[T][3])));
      tm = xrow_max(tm);
      const float mn = fmaxf(mrun, tm);
      accl *= __builtin_amdgcn_exp2f((mrun - mn) * SC2);
      mrun = mn;
      const float mo = -mrun * SC2;
#pragma unroll
      for (int i = 0; i < 7; ++i) { pq[i][0] = pq[i + 1][0]; pq[i][1] = pq[i + 1][1]; mq[i] = mq[i + 1]; }
      mq[7] = mrun;
#pragma unroll
      for (int T = 0; T < 4; ++T) {
        float p[4];
#pragma unroll
        for (int e = 0; e < 4; ++e) p[e] = __builtin_amdgcn_exp2f(fmaf(sc[T][e], SC2, mo));
        pq[7][T >> 1][(T & 1) * 2] = cvtpk(p[0], p[1]);
        pq[7][T >> 1][(T & 1) * 2 + 1] = cvtpk(p[2], p[3]);
      }
      accl = mfma(ones, pq[7][0], accl);
      accl = mfma(ones, pq[7][1], accl);
    }
  }
  float fown;
  {
    LAS float* ml = (LAS float*)(L + L_WML);
    if ((lane & 51) == 0) { ml[c * 8 + (r >> 2)] = mrun; ml[c * 8 + 4 + (r >> 2)] = accl[0]; }
    LDS_WAIT();
    CT[C_KML + c] = 1u;
#pragma unroll 1
    for (unsigned sp = 0; sp < LSPIN; ++sp) { const v4u fl = *(volatile LAS v4u*)(CT + C_KML); if (fl.x & fl.y & fl.z & fl.w) break; __builtin_amdgcn_s_sleep(1); }
    CBAR();
    float mw[4], lw[4];
#pragma unroll
    for (int w = 0; w < 4; ++w) { mw[w] = ml[w * 8 + (r >> 2)]; lw[w] = ml[w * 8 + 4 + (r >> 2)]; }
    const unsigned m24 = f24(fmaxf(fmaxf(mw[0], mw[1]), fmaxf(mw[2], mw[3])) * SC2);
    const float Mr = __uint_as_float(m24 << 8);
    float Ls = 0.f;
#pragma unroll
    for (int w = 0; w < 4; ++w) Ls = fmaf(__builtin_amdgcn_exp2f(fmaf(mw[w], SC2, -Mr)), lw[w], Ls);
    fown = __builtin_amdgcn_exp2f(fmaf(mrun, SC2, -Mr)) / Ls;
    if (c == 0 && (lane & 51) == 0)
      st16(rs, (unsigned)W_PG + (unsigned)(kap * 4 + (r >> 2)) * PGB + (unsigned)(16384 + jg * 16), v4u{m24 | TAG_LO, TAG_HI, f24(Ls) | TAG_LO, TAG_HI});
  }
  f32x4 acc[8];
#pragma unroll
  for (int m = 0; m < 8; ++m) acc[m] = f32x4{0.f, 0.f, 0.f, 0.f};
  f32x4 basea = {0.f, 0.f, 0.f, 0.f}, baseb = {0.f, 0.f, 0.f, 0.f};
  float mcur = mq[0];
#pragma unroll 1
  for (int lap = 0; lap < NTILE / 4; ++lap) {
    if (__any(mq[0] > mcur)) {
      const float al = __builtin_amdgcn_exp2f((mcur - mq[0]) * SC2);
#pragma unroll
      for (int m = 0; m < 8; ++m) acc[m] *= al;
      mcur = mq[0];
    }
    if (c == 3 && lap == NTILE / 4 - 1) {
#pragma unroll
      for (int w = 0; w < 3; ++w) {
        lds_wait_ge(CT + C_MRG + w, 1u);
        const LAS float* rw = (const LAS float*)(L + L_RING + w * SLOT);
        basea += *(const LAS f32x4*)(rw + r * 32 + 4 * g); baseb += *(const LAS f32x4*)(rw + r * 32 + 16 + 4 * g);
      }
    }
    lds_wait_ge(CT + C_LANDED, 3u + NTILE + 4u * lap + c);
    v4u vf[2][8];
    LAS unsigned char* vbase = L + L_RING + (c + 4 * (lap & 1)) * SLOT + (4 * g + (r >> 2)) * 256 + (r & 3) * 8;
#pragma unroll
    for (int ks = 0; ks < 2; ++ks)
#pragma unroll
      for (int m = 0; m < 8; ++m) {
        LAS unsigned char* p = vbase + ks * 8192 + ((m ^ vx) * 32);
        const v2u t0 = trread(p), t1 = trread(p + 4096);
        vf[ks][m] = v4u{t0.x, t0.y, t1.x, t1.y};
      }
    LDS_WAIT();
    CT[C_DONE + c] = (unsigned)(NTILE / 4 + lap + 1);
#pragma unroll
    for (int ks = 0; ks < 2; ++ks) {
      asm volatile("" : "+v"(vf[ks][0]), "+v"(vf[ks][1]), "+v"(vf[ks][2]), "+v"(vf[ks][3]) :: "memory");
      asm volatile("" : "+v"(vf[ks][4]), "+v"(vf[ks][5]), "+v"(vf[ks][6]), "+v"(vf[ks][7]) :: "memory");
    }
#pragma unroll
    for (int ks = 0; ks < 2; ++ks)
#pragma unroll
      for (int m = 0; m < 8; ++m) acc[m] = mfma(vf[ks][m], pq[0][ks], acc[m]);
#pragma unroll
    for (int i = 0; i < 7; ++i) { pq[i][0] = pq[i + 1][0]; pq[i][1] = pq[i + 1][1]; mq[i] = mq[i + 1]; }
  }
  if (c == 3) {
    const int u = r & 3;
    const f32x4 sa = u == 0 ? acc[0] : u == 1 ? acc[2] : u == 2 ? acc[4] : acc[6], sb = u == 0 ? acc[1] : u == 1 ? acc[3] : u == 2 ? acc[5] : acc[7];
    const f32x4 xa = fown * sa + basea, xb = fown * sb + baseb;
    const unsigned po = (unsigned)W_PG + (unsigned)(kap * 4 + (r >> 2)) * PGB + (unsigned)(jg * 512 + g * 64 + u * 16);
    const v2u pa = {cvtpk(xa[0], xa[1]), cvtpk(xa[2], xa[3])}, pb = {cvtpk(xb[0], xb[1]), cvtpk(xb[2], xb[3])};
    st16(rs, po, v4u{pa.x, T32, pa.y, T32});
    st16(rs, po + 256u, v4u{pb.x, T32, pb.y, T32});
    if ((r >> 2) == hh) {
      LAS unsigned char* own = L + L_RING + (jg >> 3) * SLOT + 4096 + (jg & 7) * STG_ROW + 64 * u + 8 * g;
      *(LAS v2u*)own = pa; *(LAS v2u*)(own + 32) = pb;
    }
    CT[C_PPUB] = 1u;
  } else {
    LAS float* rec = (LAS float*)(L + L_RING + c * SLOT);
    if ((r & 3) == 0) {
#pragma unroll
      for (int m = 0; m < 8; ++m) *(LAS f32x4*)(rec + (r >> 2) * 128 + 16 * m + 4 * g) = fown * acc[m];
    }
    LDS_WAIT();
    CT[C_MRG + c] = 1u;
  }
  lds_wait_ge(CT + C_WGT, 1u);
  {
    LAS unsigned* stw = (LAS unsigned*)(L + L_RING + c * SLOT + 4096) + 16 * ((lane >> 1) & 3) + 8 * (lane >> 5) + 2 * ((lane >> 3) & 3) + (lane & 1);
    gu64* pgp = (gu64*)(a.ws + W_PG + (size_t)ht * PGB + c * 4096);
    gu64* dmy = ws64 + W_QG / 8 + (size_t)bid * 256;
    unsigned need = c == (jg >> 3) ? 0xffu & ~(1u << (jg & 7)) : 0xffu, ca = 0u, cb = 0u;
    u64 va[8], vb[8];
#define H1_ISSUE(v, cm) do { cm = need; \
      _Pragma("unroll") for (int i = 0; i < 8; ++i) v[i] = __hip_atomic_load(((cm >> i) & 1u) ? pgp + i * 64 + lane : dmy, RLX_AGENT); } while (0)
#define H1_CHECK(v, cm) do { _Pragma("unroll") for (int i = 0; i < 8; ++i) \
      if (((cm & need) >> i) & 1u) { if (__all((unsigned)(v[i] >> 32) == T32)) { stw[i * (STG_ROW / 4)] = (unsigned)v[i]; need &= ~(1u << i); } } } while (0)
    v4u bw[4]; f32x4 dff = {0.f, 0.f, 0.f, 0.f};
    bool pre = false;
#define PRE_WORK() do { \
      LAS unsigned char* wb = L + L_A + (8 * g + (r >> 2)) * 128 + c * 32 + (r & 3) * 8; \
      _Pragma("unroll") for (int ks = 0; ks < 4; ++ks) { const v2u t0 = trread(wb + ks * 4096), t1 = trread(wb + ks * 4096 + 512); bw[ks] = v4u{t0.x, t0.y, t1.x, t1.y}; } \
      _Pragma("unroll") for (int ks = 0; ks < 2; ++ks) { \
        const v2u t0 = trread(wb + SLOT + ks * 4096), t1 = trread(wb + SLOT + ks * 4096 + 512); \
        const v4u hf = *(const LAS v4u*)(L + L_HB + (4 * (r & 1) + (r >> 2)) * HB_ROW + 64 * ks + 16 * g); \
        dff = mfma(hf, v4u{t0.x, t0.y, t1.x, t1.y}, dff); \
      } \
      asm volatile("" : "+v"(bw[0]), "+v"(bw[1]), "+v"(bw[2]), "+v"(bw[3]), "+v"(dff) :: "memory"); } while (0)
    lds_wait_ge(CT + C_PPUB, 1u);
    {
      const unsigned tgo = CT[C_TGO];
#pragma unroll 1
      for (int sp = 0; sp < 256 && (int)((unsigned)__builtin_amdgcn_s_memrealtime() - tgo) < 0; ++sp) __builtin_amdgcn_s_sleep(1);
    }
    H1_ISSUE(va, ca);
    _Pragma("unroll 1") for (unsigned sp = 0; sp < GSPIN; ++sp) {
      if (__builtin_popcount(need) <= H1_DIRECT) __builtin_amdgcn_s_sleep(1); else __builtin_amdgcn_s_sleep(8);
      H1_ISSUE(vb, cb);
      if (!pre && CT[C_WOK] != 0u) { CBAR(); PRE_WORK(); pre = true; }
      H1_CHECK(va, ca);
      if (need == 0u) break;
      if (__builtin_popcount(need) <= H1_DIRECT) __builtin_amdgcn_s_sleep(1); else __builtin_amdgcn_s_sleep(8);
      H1_ISSUE(va, ca);
      H1_CHECK(vb, cb);
      if (need == 0u) break;
    }
    LDS_WAIT();
    CT[C_STG + c] = 1u;
    if (!pre) { lds_wait_ge(CT + C_WOK, 1u); PRE_WORK(); }
#pragma unroll 1
    for (unsigned sp = 0; sp < LSPIN; ++sp) { const v4u fl = *(volatile LAS v4u*)(CT + C_STG); if (fl.x & fl.y & fl.z & fl.w) break; }
    CBAR();
    const LAS float* mls = (const LAS float*)(L + L_ML);
    LAS unsigned char* stg = L + L_RING + (r >> 3) * SLOT + 4096 + (r & 7) * STG_ROW + 16 * g;
    unsigned y[2];
#pragma unroll
    for (int rt = 0; rt < 2; ++rt) {
      f32x4 d = {0.f, 0.f, 0.f, 0.f};
#pragma unroll
      for (int ks = 0; ks < 4; ++ks) d = mfma(*(const LAS v4u*)(stg + 2 * rt * SLOT + 64 * ks), bw[ks], d);
      const f32x4 w = *(const LAS f32x4*)(mls + 16 * rt + 4 * g);
      y[rt] = f24(fmaf(1.f - __uint_as_float(CT[C_G + 4 * rt + g]), dff[rt], (w[0] * d[0] + w[1] * d[1]) + (w[2] * d[2] + w[3] * d[3]))) | TAG_LO;
    }
    const unsigned snd = (r & 1) ? y[0] : y[1];
    const unsigned rcv = (unsigned)__builtin_amdgcn_mov_dpp((int)snd, 0xB1, 0xf, 0xf, true);
    st16(rs, (unsigned)W_OG + (unsigned)((((4 * (r & 1) + g) * 32 + 4 * n8 + c) * 32 + ht) * 16 + (r & 14)) * 8u,
         (r & 1) ? v4u{rcv, TAG_HI, y[1], TAG_HI} : v4u{y[0], TAG_HI, rcv, TAG_HI});
  }
}
}

extern "C" void kernel_launch(void* const* d_in, const int* in_sizes, int n_in, void* d_out, int out_size, void* d_ws,
                              size_t ws_size, hipStream_t stream) {
  (void)in_sizes; (void)n_in; (void)out_size;
  if (ws_size < W_END) return;
  Args a;
  a.x = (const unsigned char*)d_in[0]; a.k = (const unsigned char*)d_in[1]; a.v = (const unsigned char*)d_in[2];
  a.wq = (const unsigned char*)d_in[3]; a.wo = (const unsigned char*)d_in[4];
  a.wg = (const unsigned char*)d_in[6]; a.wu = (const unsigned char*)d_in[7]; a.wd = (const unsigned char*)d_in[8];
  a.gw = (const unsigned char*)d_in[9];
  a.out = (unsigned char*)d_out; a.ws = (unsigned char*)d_ws;
  (void)hipFuncSetAttribute((const void*)fused_kernel, hipFuncAttributeMaxDynamicSharedMemorySize, L_BYTES);
  hipLaunchKernelGGL(fused_kernel, dim3(NWG), dim3(NTHR), L_BYTES, stream, a);
}
```
